```python
import torch

def _softmax(x, axis=-1):
    return torch.softmax(x, dim=axis)

def _gelu(x):
    return torch.nn.functional.gelu(x, approximate='tanh')

def _silu(x):
    return torch.nn.functional.silu(x)

def _sigmoid(x):
    return torch.sigmoid(x)

def _layernorm(x, axis=-1, eps=1e-05):
    m = x.mean(dim=axis, keepdim=True)
    v = x.var(dim=axis, keepdim=True, unbiased=False)
    return (x - m) / torch.sqrt(v + eps)

def _scatter_add(tgt, idx, src, axis=0):
    return tgt.clone().index_add_(axis, idx, src)

def _segment_sum(src, seg, n):
    out = torch.zeros((n,) + src.shape[1:], dtype=src.dtype)
    return out.index_add_(0, seg, src)

def _pad(x, pw):
    flat = tuple((v for pair in reversed(pw) for v in pair))
    return torch.nn.functional.pad(x, flat)
kernel_name = 'parallel_sum__ssm_conv_s4__mixer_gmlp__prompt'

def setup_inputs():

    _w = lambda a: (lambda t: t.to(torch.bfloat16) if t.is_floating_point() else t)(torch.as_tensor(a))

    def _A_setup():
        B, L, D, N = (1, 2048, 512, 128)
        A_re = -(lambda _x: torch.exp(torch.as_tensor(_x)))((lambda *s: torch.randn(*s, dtype=torch.bfloat16) * 3.2)(D, N))
        A_im = torch.pi * (lambda *s: torch.randn(*s, dtype=torch.bfloat16) * 0.1)(D, N)
        B_re = (lambda *s: torch.randn(*s, dtype=torch.bfloat16) * 0.8)(D, N)
        B_im = (lambda *s: torch.randn(*s, dtype=torch.bfloat16) * 0.8)(D, N)
        C_re = (lambda *s: torch.randn(*s, dtype=torch.bfloat16) * 0.8)(D, N)
        C_im = (lambda *s: torch.randn(*s, dtype=torch.bfloat16) * 0.8)(D, N)
        log_dt = (lambda *s: torch.randn(*s, dtype=torch.bfloat16) * 0.1)(D) * 0.5 - 4.0
        Dskip = (lambda *s: torch.randn(*s, dtype=torch.bfloat16) * 0.8)(D)
        return {'u': _w((lambda *s: torch.randn(*s, dtype=torch.bfloat16) * 0.05)(B, L, D)), 'A_re': _w(A_re), 'A_im': _w(A_im), 'B_re': _w(B_re), 'B_im': _w(B_im), 'C_re': _w(C_re), 'C_im': _w(C_im), 'log_dt': _w(log_dt), 'Dskip': _w(Dskip)}
    _A_in = _A_setup()

    def _B_setup():
        B, S, D, F = (1, 2048, 512, 1792)
        Fh = F // 2
        return {'x': _w((lambda *s: torch.randn(*s, dtype=torch.bfloat16) * 0.05)(B, S, D)), 'w_in': _w((lambda *s: torch.randn(*s, dtype=torch.bfloat16) * 0.4)(D, 2 * Fh)), 'w_sp': _w(0.01 * (lambda *s: torch.randn(*s, dtype=torch.bfloat16) * 0.4)(S, S)), 'b_sp': _w(1.0 + 0.01 * (lambda *s: torch.randn(*s, dtype=torch.bfloat16) * 0.05)(S)), 'w_out': _w((lambda *s: torch.randn(*s, dtype=torch.bfloat16) * 0.05)(Fh, D))}
    _B_in = _B_setup()
    return {'A_u': _A_in['u'], 'A_A_re': _A_in['A_re'], 'A_A_im': _A_in['A_im'], 'A_B_re': _A_in['B_re'], 'A_B_im': _A_in['B_im'], 'A_C_re': _A_in['C_re'], 'A_C_im': _A_in['C_im'], 'A_log_dt': _A_in['log_dt'], 'A_Dskip': _A_in['Dskip'], 'B_x': _B_in['x'], 'B_w_in': _B_in['w_in'], 'B_w_sp': _B_in['w_sp'], 'B_b_sp': _B_in['b_sp'], 'B_w_out': _B_in['w_out']}

def reference(A_u, A_A_re, A_A_im, A_B_re, A_B_im, A_C_re, A_C_im, A_log_dt, A_Dskip, B_x, B_w_in, B_w_sp, B_b_sp, B_w_out):

    def _A_ref(u, A_re, A_im, B_re, B_im, C_re, C_im, log_dt, Dskip):
        Bsz, L, D = u.shape
        dt = torch.exp(log_dt)[:, None]
        A = A_re + 1j * A_im
        Bc = B_re + 1j * B_im
        Cc = C_re + 1j * C_im
        logdA = dt * A
        dB = dt * Bc
        l = (lambda x, dt: x.to(dt))(torch.arange(L), A_re.dtype)
        P = torch.exp(logdA[:, :, None] * l[None, None, :])
        K = torch.einsum('dn,dnl,dn->dl', Cc, P, dB).real
        L2 = 2 * L
        Kp = _pad(K, ((0, 0), (0, L)))
        up = (lambda x, *d: x.permute(*d))(u, 0, 2, 1)
        up = _pad(up, ((0, 0), (0, 0), (0, L)))
        Kf = (lambda x, axes=(-2, -1): torch.fft.rfft2(x.float(), dim=axes))(Kp[:, None, :], axes=(-2, -1))
        Uf = (lambda x, axes=(-2, -1): torch.fft.rfft2(x.float(), dim=axes))(up[:, :, None, :], axes=(-2, -1))
        Yf = Uf * Kf[None, :, :, :]
        y = (lambda x, s, axes=(-2, -1): torch.fft.irfft2(x, s=s, dim=axes).to(torch.bfloat16))(Yf, (1, L2), axes=(-2, -1))[:, :, 0, :L]
        y = (lambda x, *d: x.permute(*d))(y, 0, 2, 1) + u * Dskip[None, None, :]
        return y

    def _B_ref(x, w_in, w_sp, b_sp, w_out):
        B, S, D = x.shape
        F = w_in.shape[-1]
        Fh = F // 2
        z = _gelu(torch.einsum('bsd,df->bsf', x, w_in))
        z1 = z[..., :Fh].clone()
        z2 = _layernorm(z[..., Fh:], axis=-1)
        g = torch.einsum('ts,bsf->btf', w_sp, z2) + b_sp[None, :, None]
        return torch.einsum('bsf,fd->bsd', z1 * g, w_out)
    x = A_u
    xn = _layernorm(x, axis=-1)
    return x + _A_ref(xn, A_A_re, A_A_im, A_B_re, A_B_im, A_C_re, A_C_im, A_log_dt, A_Dskip) + _B_ref(xn, B_w_in, B_w_sp, B_b_sp, B_w_out)
```

```cpp
#include <hip/hip_runtime.h>
#include <stdint.h>

typedef unsigned short u16;
typedef __bf16 bf16x8 __attribute__((ext_vector_type(8)));
typedef __bf16 bf16x2 __attribute__((ext_vector_type(2)));
typedef short s16x4 __attribute__((ext_vector_type(4)));
typedef short s16x8 __attribute__((ext_vector_type(8)));
typedef float f32x4 __attribute__((ext_vector_type(4)));
typedef float f32x2 __attribute__((ext_vector_type(2)));
typedef unsigned u32x4 __attribute__((ext_vector_type(4)));
typedef unsigned u32x2 __attribute__((ext_vector_type(2)));
#define LAS __attribute__((address_space(3)))
#define GAS __attribute__((address_space(1)))
typedef GAS unsigned long long gu64;
typedef GAS unsigned gu32;
#define RLX_AGENT __ATOMIC_RELAXED, __HIP_MEMORY_SCOPE_AGENT

constexpr size_t WS_XCH = 0;
constexpr size_t XCH_BYTES = 32 * 7 * 64 * 16;
constexpr size_t WS_Z1F = 1u << 20;
constexpr size_t WS_Z2F = 5u << 20;
constexpr size_t WS_XNT = 9u << 20;
constexpr size_t WS_KG = 11u << 20;
constexpr size_t WS_HF = 14u << 20;
constexpr size_t WS_YAT = 18u << 20;

struct Params {
  const u16* x; const u16* a_re; const u16* a_im; const u16* b_re; const u16* b_im; const u16* c_re; const u16* c_im;
  const u16* log_dt; const u16* dskip; const u16* w_in; const u16* w_sp; const u16* b_sp; const u16* w_out;
  u16* out; char* ws;
};

__device__ __forceinline__ float bf2f(unsigned h) { return __builtin_bit_cast(float, h << 16); }
__device__ __forceinline__ float bflo(unsigned w) { return __builtin_bit_cast(float, w << 16); }
__device__ __forceinline__ float bfhi(unsigned w) { return __builtin_bit_cast(float, w & 0xffff0000u); }
__device__ __forceinline__ unsigned pk2(float lo, float hi) { bf16x2 v = {(__bf16)lo, (__bf16)hi}; return __builtin_bit_cast(unsigned, v); }
__device__ __forceinline__ float rb(float v) { return (float)(__bf16)v; }
template <int CTRL> __device__ __forceinline__ float dpp_add(float v) {
  return v + __builtin_bit_cast(float, __builtin_amdgcn_update_dpp(0, __builtin_bit_cast(int, v), CTRL, 0xf, 0xf, true));
}
__device__ __forceinline__ float allred16(float v) { v = dpp_add<0xB1>(v); v = dpp_add<0x4E>(v); v = dpp_add<0x141>(v); v = dpp_add<0x140>(v); return v; }
__device__ __forceinline__ float gelu_tanh(float v) {
  const float t = v * v;
  const float u = v * __builtin_fmaf(t, 0.044715f, 1.0f);
  const float e = __builtin_amdgcn_exp2f(u * (-2.0f * 0.7978845608028654f * 1.4426950408889634f));
  return v * __builtin_amdgcn_rcpf(1.0f + e);
}
#define GLDS16(g, l) __builtin_amdgcn_global_load_lds((const unsigned*)(g), (LAS unsigned*)(l), 16, 0, 0)
#define GLDS4(g, l) __builtin_amdgcn_global_load_lds((const unsigned*)(g), (LAS unsigned*)(l), 4, 0, 0)
#define WAIT_V(n) asm volatile("s_waitcnt vmcnt(%0)" ::"i"(n) : "memory")
#define BAR() do { asm volatile("s_waitcnt lgkmcnt(0)" ::: "memory"); __builtin_amdgcn_s_barrier(); asm volatile("" ::: "memory"); } while (0)
__device__ __forceinline__ f32x4 mfma16(bf16x8 a, bf16x8 b, f32x4 c) { return __builtin_amdgcn_mfma_f32_16x16x32_bf16(a, b, c, 0, 0, 0); }
__device__ __forceinline__ s16x4 trread(LAS char* a) { return __builtin_amdgcn_ds_read_tr16_b64_v4i16((LAS s16x4*)a); }
#define CAS __attribute__((address_space(4)))
__device__ __forceinline__ unsigned ldsaddr(LAS char* a) { return (unsigned)(uintptr_t)a; }
#define TRREAD_ASM(dst, addr, off) asm volatile("ds_read_b64_tr_b16 %0, %1 offset:%2" : "=v"(dst) : "v"(addr), "i"(off) : "memory")
__device__ __forceinline__ bf16x8 cat8(s16x4 lo, s16x4 hi) { return __builtin_bit_cast(bf16x8, __builtin_shufflevector(lo, hi, 0, 1, 2, 3, 4, 5, 6, 7)); }

constexpr int K1_RING = 65536, K1_STG = 16384, K1_SCR = K1_RING + 3 * K1_STG, K1_PAR = K1_RING + 5 * K1_STG  , K1_T = K1_PAR + 3072,
              K1_LDS = K1_T + 2 * 4352  ;
constexpr int K1_P = K1_PAR, K1_S = K1_PAR + 2048;

__global__ __launch_bounds__(512, 2) void k1_kernel(Params p) {
  extern __shared__ __attribute__((aligned(16))) char smem[];
  LAS char* lds = (LAS char*)smem;
  const int tid = threadIdx.x, lane = tid & 63, w = __builtin_amdgcn_readfirstlane(tid >> 6);
  const int b = blockIdx.x, xcd = b & 7, idx = b >> 3;
  const bool gemm = idx < 28;
  const int ib = 4 * xcd + (idx & 3), jb = idx >> 2;
  const int fr = lane & 15, g = lane >> 4;
  const int rot = 2 * (ib & 3);

  if (w < 6) {
    const u16* arr = w == 0 ? p.a_re : w == 1 ? p.a_im : w == 2 ? p.b_re : w == 3 ? p.b_im : w == 4 ? p.c_re : p.c_im;
#pragma unroll
    for (int ch = 0; ch < 2; ++ch) GLDS4(arr + (size_t)(2 * b + ch) * 128 + lane * 2, lds + K1_PAR + (ch * 6 + w) * 256);
  }
  unsigned voffB[2];
#pragma unroll
  for (int nn = 0; nn < 2; ++nn) {
    const int n = 2 * w + nn, row = 4 * n + g, chunk = fr ^ ((g << 2) | (n & 3));
    voffB[nn] = (unsigned)(row * 1792) * 2u + (unsigned)chunk * 16u;
  }
  auto stageB = [&](int s) {
    const int half = s >> 3, kst = ((s & 7) + rot) & 7;
    const char* base = (const char*)p.w_in + ((size_t)(64 * kst) * 1792 + (half == 0 ? 896 : 0) + 128 * jb) * 2;
    LAS char* dst = lds + K1_RING + (s % 5) * K1_STG + w * 2048;
    GLDS16(base + voffB[0], dst);
    GLDS16(base + voffB[1], dst + 1024);
  };
  if (gemm) {
#pragma unroll
    for (int n = 0; n < 8; ++n) {
      const int r = 8 * w + ((n + jb) & 7);
      GLDS16(p.x + (size_t)(64 * ib + r) * 512 + lane * 8, lds + r * 1024);
    }
    stageB(0); stageB(1); stageB(2);
    WAIT_V(14);
  } else {
    WAIT_V(0);
  }
  BAR();

  {
    const int ch = w >> 2, kq = w & 3, d = 2 * b + ch;
    const unsigned ld2 = *(const CAS unsigned*)(p.log_dt + 2 * b);
    const float dt = rb(expf(ch ? bfhi(ld2) : bflo(ld2)));
    const float a0 = (float)((fr & 7) + 128 * (fr >> 3));
    const float a1 = (float)(fr < 8 ? 256 + 2 * (fr & 3) + 128 * (fr >> 2) : 512 + 4 * (fr & 1) + 128 * ((fr - 8) >> 1));
    const float a2 = (float)(fr < 8 ? 1024 + 128 * fr : (fr == 8 ? 2048 : 0));
    const float bb = (float)(8 * fr);
    f32x4 acc[3] = {{0, 0, 0, 0}, {0, 0, 0, 0}, {0, 0, 0, 0}};
#pragma unroll
    for (int kk = 0; kk < 2; ++kk) {
      const int n0 = 16 * (2 * kq + kk) + 4 * g;
      u32x2 pr[6];
#pragma unroll
      for (int a = 0; a < 6; ++a) pr[a] = *(LAS u32x2*)(lds + K1_PAR + (ch * 6 + a) * 256 + n0 * 2);
      u32x4 A0, A1, A2, Bv;
#pragma unroll
      for (int i = 0; i < 4; ++i) {
        float v[6];
#pragma unroll
        for (int a = 0; a < 6; ++a) v[a] = (i & 1) ? bfhi(pr[a][i >> 1]) : bflo(pr[a][i >> 1]);
        const float lr = dt * v[0] * 1.4426950408889634f;
        const float fq = dt * v[1] * 0.15915494309189535f;
        const float dbr = dt * v[2], dbi = dt * v[3];
        const float wr = v[4] * dbr - v[5] * dbi, wi = v[4] * dbi + v[5] * dbr;
        auto cx = [&](float a, float& re, float& im) {
          const float m = __builtin_amdgcn_exp2f(lr * a);
          const float ph = __builtin_amdgcn_fractf(fq * a);
          re = m * __builtin_amdgcn_cosf(ph); im = m * __builtin_amdgcn_sinf(ph);
        };
        float re, im;
        cx(a0, re, im); A0[i] = pk2(re, im);
        cx(a1, re, im); A1[i] = pk2(re, im);
        cx(a2, re, im); A2[i] = pk2(re, im);
        cx(bb, re, im); Bv[i] = pk2(wr * re - wi * im, -(wr * im + wi * re));
      }
      const bf16x8 Bf = __builtin_bit_cast(bf16x8, Bv);
      acc[0] = mfma16(__builtin_bit_cast(bf16x8, A0), Bf, acc[0]);
      acc[1] = mfma16(__builtin_bit_cast(bf16x8, A1), Bf, acc[1]);
      acc[2] = mfma16(__builtin_bit_cast(bf16x8, A2), Bf, acc[2]);
    }
    LAS f32x4* scr = (LAS f32x4*)(lds + K1_SCR);
#pragma unroll
    for (int t = 0; t < 3; ++t) scr[((ch * 3 + t) * 4 + kq) * 64 + lane] = acc[t];
    BAR();
    if (kq < 3) {
      const int t = kq;
      f32x4 s = scr[((ch * 3 + t) * 4 + 0) * 64 + lane];
#pragma unroll
      for (int k2 = 1; k2 < 4; ++k2) s += scr[((ch * 3 + t) * 4 + k2) * 64 + lane];
      LAS u16* T = (LAS u16*)(lds + K1_T + ch * 4352);
#pragma unroll
      for (int r = 0; r < 4; ++r) {
        const int row = 4 * g + r;
        const int a = t == 0 ? (row & 7) + 128 * (row >> 3)
                    : t == 1 ? (row < 8 ? 256 + 2 * (row & 3) + 128 * (row >> 2) : 512 + 4 * (row & 1) + 128 * ((row - 8) >> 1))
                             : (row < 8 ? 1024 + 128 * row : 2048);
        if (t < 2 || row <= 8) T[a + 8 * fr] = (u16)(pk2(s[r], 0.f) & 0xffffu);
      }
    }
    BAR();
    {
      const int tt = tid & 255;
      LAS u16* T = (LAS u16*)(lds + K1_T + ch * 4352);
      u32x4 o;
#pragma unroll
      for (int e = 0; e < 8; e += 2) {
        const int l0 = 2047 - (8 * tt + e), l1 = l0 - 1;
        const int i0 = (int)rb((float)l0), i1 = (int)rb((float)l1);
        o[e >> 1] = (unsigned)T[i0] | ((unsigned)T[i1] << 16);
      }
      *(u32x4*)(p.ws + WS_KG + (size_t)d * 4096 + tt * 16) = o;
    }
  }
  if (!gemm) return;

  WAIT_V(6);
#pragma unroll
  for (int rq = 0; rq < 2; ++rq) {
    const int row = 8 * w + 4 * rq + g;
    u32x4 v[4];
#pragma unroll
    for (int i = 0; i < 4; ++i) v[i] = *(LAS u32x4*)(lds + row * 1024 + (16 * i + fr) * 16);
    float s = 0.f;
#pragma unroll
    for (int i = 0; i < 4; ++i)
#pragma unroll
      for (int e = 0; e < 4; ++e) s += bflo(v[i][e]) + bfhi(v[i][e]);
    const float mean = allred16(s) * (1.0f / 512.0f);
    float q = 0.f;
#pragma unroll
    for (int i = 0; i < 4; ++i)
#pragma unroll
      for (int e = 0; e < 4; ++e) { const float d0 = bflo(v[i][e]) - mean, d1 = bfhi(v[i][e]) - mean; q += d0 * d0 + d1 * d1; }
    const float var = allred16(q) * (1.0f / 512.0f);
    const float m = rb(mean), den = rb(__builtin_sqrtf(rb(rb(var) + 1e-5f))), rd = 1.0f / den;
#pragma unroll
    for (int i = 0; i < 4; ++i) {
      u32x4 o;
#pragma unroll
      for (int e = 0; e < 4; ++e) o[e] = pk2(rb(bflo(v[i][e]) - m) * rd, rb(bfhi(v[i][e]) - m) * rd);
      *(LAS u32x4*)(lds + row * 1024 + (16 * i + (fr ^ (row & 15))) * 16) = o;
    }
  }
  BAR();
  stageB(3);
  const int wm = w & 1, wn = w >> 1;
  bf16x8 af[2][16];
#pragma unroll
  for (int s = 0; s < 8; ++s)
#pragma unroll
    for (int kk = 0; kk < 2; ++kk) {
      const int ks = 2 * ((s + rot) & 7) + kk, chunk = 4 * ks + g;
#pragma unroll
      for (int rt = 0; rt < 2; ++rt)
        af[rt][2 * s + kk] = *(LAS bf16x8*)(lds + (32 * wm + 16 * rt + fr) * 1024 + ((chunk & ~15) | ((chunk & 15) ^ fr)) * 16);
    }
  {
    const int nblk = jb < 4 ? 5 : 4;
    for (int u = w; u < 2 * nblk; u += 8) {
      const int dblk = jb + 7 * (u >> 1), t0 = 32 * (u & 1) + 8 * g;
      const int chunk = 2 * dblk + ((fr & 3) >> 1);
      s16x4 h[2];
#pragma unroll
      for (int tt = 0; tt < 2; ++tt) {
        const int row = t0 + 4 * tt + (fr >> 2);
        const unsigned ad = ldsaddr(lds + row * 1024 + ((chunk & ~15) | ((chunk & 15) ^ (row & 15))) * 16 + 8 * (fr & 1));
        TRREAD_ASM(h[tt], ad, 0);
      }
      asm volatile("s_waitcnt lgkmcnt(0)" : "+v"(h[0]), "+v"(h[1])::"memory");
      *(bf16x8*)(p.ws + WS_XNT + ((size_t)(16 * dblk + fr) * 2048 + 64 * ib + t0) * 2) = cat8(h[0], h[1]);
    }
  }

  unsigned boff[2][2];
#pragma unroll
  for (int c = 0; c < 2; ++c)
#pragma unroll
    for (int t = 0; t < 2; ++t) {
      const int row = 8 * g + 4 * t + (fr >> 2), chn = 2 * (2 * wn + c) + ((fr & 3) >> 1);
      boff[c][t] = ldsaddr(lds + K1_RING) + 256 * row + 16 * (chn ^ (((fr >> 2) << 2) | ((2 * g + t) & 3))) + 8 * (fr & 1);
    }
  f32x4 acc[2][2];
  float z2v[2][2][4];
#pragma unroll
  for (int s = 0; s < 16; ++s) {
    if (s == 0 || s == 8) {
#pragma unroll
      for (int rt = 0; rt < 2; ++rt)
#pragma unroll
        for (int c = 0; c < 2; ++c) acc[rt][c] = (f32x4){0, 0, 0, 0};
    }
    if (s <= 12) WAIT_V(6); else if (s == 13) WAIT_V(4); else if (s == 14) WAIT_V(2); else WAIT_V(0);
    BAR();
    if (s + 4 < 16) stageB(s + 4);
    {
      s16x4 tr[2][2][2];
#pragma unroll
      for (int kk = 0; kk < 2; ++kk)
#pragma unroll
        for (int c = 0; c < 2; ++c)
#pragma unroll
          for (int t = 0; t < 2; ++t) { const unsigned ad = boff[c][t] + (s % 5) * K1_STG; TRREAD_ASM(tr[kk][c][t], ad, kk * 8192); }
#pragma unroll
      for (int kk = 0; kk < 2; ++kk) {
        if (kk == 0) asm volatile("s_waitcnt lgkmcnt(4)" : "+v"(tr[0][0][0]), "+v"(tr[0][0][1]), "+v"(tr[0][1][0]), "+v"(tr[0][1][1])::"memory");
        else asm volatile("s_waitcnt lgkmcnt(0)" : "+v"(tr[1][0][0]), "+v"(tr[1][0][1]), "+v"(tr[1][1][0]), "+v"(tr[1][1][1])::"memory");
        bf16x8 bf[2];
#pragma unroll
        for (int c = 0; c < 2; ++c) bf[c] = cat8(tr[kk][c][0], tr[kk][c][1]);
#pragma unroll
        for (int rt = 0; rt < 2; ++rt)
#pragma unroll
          for (int c = 0; c < 2; ++c) {
            if (s < 8) acc[rt][c] = mfma16(af[rt][2 * (s & 7) + kk], bf[c], acc[rt][c]);
            else acc[rt][c] = mfma16(bf[c], af[rt][2 * (s & 7) + kk], acc[rt][c]);
          }
      }
    }
    if (s == 7) {
      LAS f32x2* P = (LAS f32x2*)(lds + K1_P);
#pragma unroll
      for (int rt = 0; rt < 2; ++rt)
#pragma unroll
        for (int r = 0; r < 4; ++r) {
          const float z0 = rb(gelu_tanh(rb(acc[rt][0][r]))), z1 = rb(gelu_tanh(rb(acc[rt][1][r])));
          z2v[rt][0][r] = z0; z2v[rt][1][r] = z1;
          const float mw = allred16(z0 + z1) * (1.0f / 32.0f);
          const float d0 = z0 - mw, d1 = z1 - mw;
          const float q2 = allred16(d0 * d0 + d1 * d1);
          if (fr == 0) P[(32 * wm + 16 * rt + 4 * g + r) * 4 + wn] = (f32x2){mw, q2};
        }
      BAR();
      if (w == 0) {
        const f32x2 a = P[lane * 4 + 0], bq = P[lane * 4 + 1], c = P[lane * 4 + 2], dq = P[lane * 4 + 3];
        const float mj = (a[0] + bq[0] + c[0] + dq[0]) * 0.25f;
        const float da = a[0] - mj, db = bq[0] - mj, dc = c[0] - mj, dd = dq[0] - mj;
        const float m2 = (a[1] + bq[1]) + (c[1] + dq[1]) + 32.0f * ((da * da + db * db) + (dc * dc + dd * dd));
        gu64* slot = (gu64*)(p.ws + WS_XCH) + ((size_t)(ib * 7 + jb) * 64 + lane) * 2;
        __hip_atomic_store(slot, (1ull << 32) | __builtin_bit_cast(unsigned, mj), RLX_AGENT);
        __hip_atomic_store(slot + 1, (1ull << 32) | __builtin_bit_cast(unsigned, m2), RLX_AGENT);
      }
    }
  }
#pragma unroll
  for (int rt = 0; rt < 2; ++rt) {
    u32x4 o;
#pragma unroll
    for (int c = 0; c < 2; ++c) {
      float z[4];
#pragma unroll
      for (int r = 0; r < 4; ++r) z[r] = gelu_tanh(rb(acc[rt][c][r]));
      o[2 * c] = pk2(z[0], z[1]); o[2 * c + 1] = pk2(z[2], z[3]);
    }
    const int t16 = 4 * ib + 2 * wm + rt, cp = 4 * jb + wn;
    *(u32x4*)(p.ws + WS_Z1F + ((size_t)(t16 * 28 + cp) * 64 + lane) * 16) = o;
  }
  LAS f32x2* S = (LAS f32x2*)(lds + K1_S);
  if (w == 0) {
    gu64* slot = (gu64*)(p.ws + WS_XCH) + ((size_t)(ib * 7) * 64 + lane) * 2;
    float mj[7], m2j[7];
    for (unsigned spins = 0; spins < (1u << 20); ++spins) {
      bool ok = true;
#pragma unroll
      for (int j = 0; j < 7; ++j) {
        const unsigned long long u0 = __hip_atomic_load(slot + j * 128, RLX_AGENT), u1 = __hip_atomic_load(slot + j * 128 + 1, RLX_AGENT);
        mj[j] = __builtin_bit_cast(float, (unsigned)u0); m2j[j] = __builtin_bit_cast(float, (unsigned)u1);
        ok &= ((u0 >> 32) == 1ull) & ((u1 >> 32) == 1ull);
      }
      if (__all(ok)) break;
      __builtin_amdgcn_s_sleep(1);
    }
    float mean = 0.f;
#pragma unroll
    for (int j = 0; j < 7; ++j) mean += mj[j];
    mean *= (1.0f / 7.0f);
    float m2 = 0.f;
#pragma unroll
    for (int j = 0; j < 7; ++j) { const float dm = mj[j] - mean; m2 += m2j[j] + 128.0f * dm * dm; }
    const float var = m2 * (1.0f / 896.0f);
    const float den = rb(__builtin_sqrtf(rb(rb(var) + 1e-5f)));
    S[lane] = (f32x2){rb(mean), 1.0f / den};
  }
  BAR();
  {
    f32x4 mm[2], rr[2];
#pragma unroll
    for (int rt = 0; rt < 2; ++rt)
#pragma unroll
      for (int r = 0; r < 4; ++r) { const f32x2 sv = S[32 * wm + 16 * rt + 4 * g + r]; mm[rt][r] = sv[0]; rr[rt][r] = sv[1]; }
#pragma unroll
    for (int c = 0; c < 2; ++c) {
      u32x4 o;
#pragma unroll
      for (int rt = 0; rt < 2; ++rt) {
        float z[4];
#pragma unroll
        for (int r = 0; r < 4; ++r) z[r] = rb(z2v[rt][c][r] - mm[rt][r]) * rr[rt][r];
        o[2 * rt] = pk2(z[0], z[1]); o[2 * rt + 1] = pk2(z[2], z[3]);
      }
      const int ks = 2 * ib + wm, c16 = 8 * jb + 2 * wn + c;
      *(u32x4*)(p.ws + WS_Z2F + ((size_t)(ks * 56 + c16) * 64 + lane) * 16) = o;
    }
  }
}

constexpr int K2_STG = 22528, K2_NBUF = 3, K2_RC = K2_NBUF * K2_STG  , K2_RCS = 4160, K2_RCCH = 8 * K2_RCS  ,
              K2_U = K2_RC + 2 * K2_RCCH  , K2_LDS = K2_U + 2 * 4608  ;

template <int NC>
__device__ __forceinline__ void g2_loop(LAS char* lds, const char* gA, const char* gB, int w, int lane, int wm, int wk, int c0, int rot, f32x4 (&acc)[2][4]) {
  constexpr int D = K2_NBUF - 1;
  const bool three = (w < 6);
  auto stage = [&](int st, int buf) {
    const int sr = (st + rot) & 31;
    LAS char* base = lds + buf * K2_STG;
    GLDS16(gA + (size_t)sr * 128, base + w * 1024);
    const int f0 = w, f1 = w + 8;
    GLDS16(gB + ((size_t)(2 * sr + (f0 >= 7)) * 56 + (f0 >= 7 ? f0 - 7 : f0)) * 1024, base + 8192 + f0 * 1024);
    if (three) GLDS16(gB + ((size_t)(2 * sr + 1) * 56 + (f1 - 7)) * 1024, base + 8192 + f1 * 1024);
  };
  const int q = lane >> 4, fr = lane & 15;
  int aoff[2][2];
#pragma unroll
  for (int r = 0; r < 2; ++r)
#pragma unroll
    for (int pp = 0; pp < 2; ++pp) {
      const int row = 16 * (2 * wm + r) + fr, chunk = 4 * wk + 2 * pp + (q >> 1);
      aoff[r][pp] = row * 128 + ((chunk ^ ((row >> 1) & 7)) * 16) + (q & 1) * 8;
      asm volatile("" : "+v"(aoff[r][pp]));
    }
  const int boff = 8192 + (wk * 7 + c0) * 1024 + lane * 16;
  int buf = 0, nbuf = D;
  for (int st = 0; st < 32; ++st) {
    if (st <= 32 - D) { if (three) WAIT_V(3 * (D - 1)); else WAIT_V(2 * (D - 1)); }
    else WAIT_V(0);
    BAR();
    if (st + D < 32) stage(st + D, nbuf);
    LAS char* base = lds + buf * K2_STG;
    bf16x8 a[2], bb[NC];
#pragma unroll
    for (int r = 0; r < 2; ++r) a[r] = cat8(*(LAS s16x4*)(base + aoff[r][0]), *(LAS s16x4*)(base + aoff[r][1]));
#pragma unroll
    for (int c = 0; c < NC; ++c) bb[c] = *(LAS bf16x8*)(base + boff + c * 1024);
#pragma unroll
    for (int r = 0; r < 2; ++r)
#pragma unroll
      for (int c = 0; c < NC; ++c) acc[r][c] = mfma16(bb[c], a[r], acc[r][c]);
    buf = (buf + 1 == K2_NBUF) ? 0 : buf + 1;
    nbuf = (nbuf + 1 == K2_NBUF) ? 0 : nbuf + 1;
  }
}

__global__ __launch_bounds__(512, 2) void k2_kernel(Params p) {
  extern __shared__ __attribute__((aligned(16))) char smem[];
  LAS char* lds = (LAS char*)smem;
  const int tid = threadIdx.x, lane = tid & 63, w = __builtin_amdgcn_readfirstlane(tid >> 6);
  const int b = blockIdx.x, xcd = b & 7, idx = b >> 3;
  const int ib = (xcd >> 1) * 8 + (idx & 7), jb = (xcd & 1) * 4 + (idx >> 3);
  const int rot = (4 * (idx & 7) + 8 * (idx >> 3)) & 31;
  const int fr = lane & 15, g = lane >> 4;
  const int ch = w >> 2, hq = w & 3, tt = tid & 255, d = 2 * b + ch;

  u32x4 g0, g1;
  {
    const char* gp = p.ws + WS_KG + (size_t)d * 4096 + tt * 16;
    const char* gp1 = tt < 255 ? gp + 16 : gp;
    asm volatile("global_load_dwordx4 %0, %1, off" : "=v"(g0) : "v"(gp) : "memory");
    asm volatile("global_load_dwordx4 %0, %1, off" : "=v"(g1) : "v"(gp1) : "memory");
  }
  GLDS16(p.ws + WS_XNT + ((size_t)d * 2048 + hq * 512) * 2 + lane * 16, lds + K2_U + ch * 4608 + 512 + hq * 1024);
  const int arow = 8 * w + (lane >> 3);
  const char* gA = (const char*)p.w_sp + ((size_t)(64 * ib + arow) * 2048) * 2 + (((lane & 7) ^ ((arow >> 1) & 7)) * 16);
  const char* gB = p.ws + WS_Z2F + (size_t)(7 * jb) * 1024 + lane * 16;
  {
    const bool three = (w < 6);
#pragma unroll
    for (int st = 0; st < K2_NBUF - 1; ++st) {
      const int sr = (st + rot) & 31;
      LAS char* base = lds + st * K2_STG;
      GLDS16(gA + (size_t)sr * 128, base + w * 1024);
      const int f0 = w, f1 = w + 8;
      GLDS16(gB + ((size_t)(2 * sr + (f0 >= 7)) * 56 + (f0 >= 7 ? f0 - 7 : f0)) * 1024, base + 8192 + f0 * 1024);
      if (three) GLDS16(gB + ((size_t)(2 * sr + 1) * 56 + (f1 - 7)) * 1024, base + 8192 + f1 * 1024);
    }
    if (tt < 32) *(LAS u32x4*)(lds + K2_U + ch * 4608 + tt * 16) = (u32x4){0, 0, 0, 0};
    asm volatile("s_waitcnt vmcnt(%2)" : "+v"(g0), "+v"(g1) : "i"(1 + 2 * (K2_NBUF - 1)) : "memory");
    if (tt == 255) g1 = (u32x4){0, 0, 0, 0};
    const unsigned wd[8] = {g0[0], g0[1], g0[2], g0[3], g1[0], g1[1], g1[2], g1[3]};
    LAS char* rc = lds + K2_RC + ch * K2_RCCH;
#pragma unroll
    for (int c = 0; c < 8; ++c) {
      const int s = 7 - c;
      u32x4 o;
#pragma unroll
      for (int i = 0; i < 4; ++i) o[i] = (s & 1) ? __builtin_amdgcn_alignbit(wd[(s >> 1) + i + 1], wd[(s >> 1) + i], 16) : wd[(s >> 1) + i];
      *(LAS u32x4*)(rc + c * K2_RCS + tt * 16) = o;
    }
    if (tt < 8) *(LAS u32x4*)(rc + tt * K2_RCS + 4096) = (u32x4){0, 0, 0, 0};
    if (three) WAIT_V(3 * (K2_NBUF - 1)); else WAIT_V(2 * (K2_NBUF - 1));
  }
  BAR();
  {
    f32x4 cacc[8];
#pragma unroll
    for (int r = 0; r < 8; ++r) cacc[r] = (f32x4){0, 0, 0, 0};
    LAS char* ub = lds + K2_U + ch * 4608 + (240 + 16 * fr + 8 * g) * 2;
    LAS char* kb = lds + K2_RC + ch * K2_RCCH + (fr & 7) * K2_RCS + (2024 + 8 * g - 8 * (fr >> 3)) * 2;
#pragma unroll
    for (int pp = 0; pp < 2; ++pp) {
      const int p0 = 2 * hq + pp;
      bf16x8 ua[8];
#pragma unroll
      for (int r = 0; r < 8; ++r) ua[r] = *(LAS bf16x8*)(ub + (8 * r - p0) * 64);
#pragma unroll
      for (int i = 0; i < 8; ++i) {
        const bf16x8 kf = *(LAS bf16x8*)(kb - (p0 + 8 * i) * 64);
#pragma unroll
        for (int rt = i; rt < 8; ++rt) cacc[rt] = mfma16(ua[rt - i], kf, cacc[rt]);
      }
    }
    BAR();
    LAS float* part = (LAS float*)(lds + K2_RC + (ch * 4 + hq) * 8192);
#pragma unroll
    for (int rt = 0; rt < 8; ++rt)
#pragma unroll
      for (int r = 0; r < 4; ++r) part[256 * rt + 64 * g + 16 * r + fr] = cacc[rt][r];
    BAR();
    {
      f32x4 s0 = {0, 0, 0, 0}, s1 = {0, 0, 0, 0};
#pragma unroll
      for (int h = 0; h < 4; ++h) {
        LAS f32x4* pp = (LAS f32x4*)(lds + K2_RC + (ch * 4 + h) * 8192 + tt * 32);
        s0 += pp[0]; s1 += pp[1];
      }
      const u32x4 uv = *(LAS u32x4*)(lds + K2_U + ch * 4608 + 512 + tt * 16);
      const unsigned dk2 = *(const CAS unsigned*)(p.dskip + 2 * b);
      const float dsk = ch ? bfhi(dk2) : bflo(dk2);
      u32x4 o;
#pragma unroll
      for (int e = 0; e < 4; ++e) {
        const float y0 = e < 2 ? s0[2 * e] : s1[2 * e - 4], y1 = e < 2 ? s0[2 * e + 1] : s1[2 * e - 3];
        o[e] = pk2(rb(y0) + rb(bflo(uv[e]) * dsk), rb(y1) + rb(bfhi(uv[e]) * dsk));
      }
      *(u32x4*)(p.ws + WS_YAT + (size_t)d * 4096 + tt * 16) = o;
    }
  }
  const int wn = w >> 2, wm = w & 1, wk = (w >> 1) & 1;
  f32x4 acc[2][4];
#pragma unroll
  for (int r = 0; r < 2; ++r)
#pragma unroll
    for (int c = 0; c < 4; ++c) acc[r][c] = (f32x4){0, 0, 0, 0};
  if (wn == 0) g2_loop<4>(lds, gA, gB, w, lane, wm, wk, 0, rot, acc);
  else g2_loop<3>(lds, gA, gB, w, lane, wm, wk, 4, rot, acc);
  BAR();
  LAS f32x4* red = (LAS f32x4*)lds;
  if (wk) {
#pragma unroll
    for (int c = 0; c < 4; ++c) { const f32x4 tmp = acc[0][c]; acc[0][c] = acc[1][c]; acc[1][c] = tmp; asm volatile("" : "+v"(acc[0][c]), "+v"(acc[1][c])); }
  }
#pragma unroll
  for (int c = 0; c < 4; ++c) red[(((wn * 2 + wm) * 2 + wk) * 4 + c) * 64 + lane] = acc[1][c];
  BAR();
  {
    const int rt = 2 * wm + wk, nc = wn == 0 ? 4 : 3;
    const int t = 64 * ib + 16 * rt + fr;
    const float bs = bf2f(p.b_sp[t]);
    const int t16 = 4 * ib + rt;
    unsigned hw[4][2];
#pragma unroll
    for (int c = 0; c < 4; ++c) {
      if (c < nc) {
        const f32x4 v = acc[0][c] + red[(((wn * 2 + wm) * 2 + (wk ^ 1)) * 4 + c) * 64 + lane];
        const int c16 = 7 * jb + 4 * wn + c;
        const u32x2 z = *(const u32x2*)(p.ws + WS_Z1F + ((size_t)(t16 * 28 + (c16 >> 1)) * 64 + lane) * 16 + (c16 & 1) * 8);
        const float h0 = bflo(z[0]) * rb(rb(v[0]) + bs), h1 = bfhi(z[0]) * rb(rb(v[1]) + bs);
        const float h2 = bflo(z[1]) * rb(rb(v[2]) + bs), h3 = bfhi(z[1]) * rb(rb(v[3]) + bs);
        hw[c][0] = pk2(h0, h1); hw[c][1] = pk2(h2, h3);
      } else { hw[c][0] = 0; hw[c][1] = 0; }
    }
    char* hb = p.ws + WS_HF + ((size_t)(ib * 8 + jb) * 4 + rt) * 3584;
    if (wn == 0) {
      *(u32x4*)(hb + lane * 16) = (u32x4){hw[0][0], hw[0][1], hw[1][0], hw[1][1]};
      *(u32x4*)(hb + 1024 + lane * 16) = (u32x4){hw[2][0], hw[2][1], hw[3][0], hw[3][1]};
    } else {
      *(u32x4*)(hb + 2048 + lane * 16) = (u32x4){hw[0][0], hw[0][1], hw[1][0], hw[1][1]};
      *(u32x2*)(hb + 3072 + lane * 8) = (u32x2){hw[2][0], hw[2][1]};
    }
  }
}

constexpr int K3_W = 0, K3_Y = 896 * 128  , K3_LDS = K3_Y + 8192;

__global__ __launch_bounds__(512, 2) void k3_kernel(Params p) {
  extern __shared__ __attribute__((aligned(16))) char smem[];
  LAS char* lds = (LAS char*)smem;
  const int tid = threadIdx.x, lane = tid & 63, w = __builtin_amdgcn_readfirstlane(tid >> 6);
  const int b = blockIdx.x, xcd = b & 7, idx = b >> 3;
  const int ib = (xcd >> 1) * 8 + (idx & 7), jd = (xcd & 1) * 4 + (idx >> 3);
  const int fr = lane & 15, g = lane >> 4;
  const int wm = w & 1, wk = w >> 1;
  {
    const int r8 = lane >> 3;
    const char* src = (const char*)p.w_out + (size_t)r8 * 1024 + jd * 128 + (((lane & 7) ^ (2 * ((r8 >> 1) & 3))) * 16);
#pragma unroll
    for (int n = 0; n < 14; ++n) { const int pc = w * 14 + n; GLDS16(src + (size_t)pc * 8192, lds + K3_W + pc * 1024); }
    const char* ys = p.ws + WS_YAT + ((size_t)(64 * jd + 8 * w + r8) * 2048 + 64 * ib) * 2 + (((lane & 7) ^ (2 * ((r8 >> 1) & 3))) * 16);
    GLDS16(ys, lds + K3_Y + w * 1024);
  }
  u32x4 hf[2][2][3]; u32x2 hh[2][2];
#pragma unroll
  for (int r = 0; r < 2; ++r)
#pragma unroll
    for (int s = 0; s < 2; ++s) {
      const char* hb = p.ws + WS_HF + ((size_t)(ib * 8 + 2 * wk + s) * 4 + 2 * wm + r) * 3584;
#pragma unroll
      for (int m = 0; m < 3; ++m) hf[r][s][m] = *(const u32x4*)(hb + m * 1024 + lane * 16);
      hh[r][s] = *(const u32x2*)(hb + 3072 + lane * 8);
    }
  WAIT_V(0);
  BAR();
  f32x4 acc[2][4];
#pragma unroll
  for (int r = 0; r < 2; ++r)
#pragma unroll
    for (int c = 0; c < 4; ++c) acc[r][c] = (f32x4){0, 0, 0, 0};
  auto wadr = [&](int frow, int c) {
    const int row = frow + 4 * g + (fr >> 2);
    const int col = 32 * (c >> 1) + 8 * (fr & 3) + 4 * (c & 1);
    const int chunk = col >> 3;
    return lds + K3_W + row * 128 + ((chunk ^ (2 * ((row >> 1) & 3))) * 16) + (col & 4) * 2;
  };
#pragma unroll
  for (int s = 0; s < 2; ++s) {
    const int f0 = 112 * (2 * wk + s);
#pragma unroll
    for (int m = 0; m < 3; ++m)
#pragma unroll
      for (int c = 0; c < 4; ++c) {
        const bf16x8 wf = cat8(trread(wadr(f0 + 32 * m, c)), trread(wadr(f0 + 32 * m + 16, c)));
#pragma unroll
        for (int r = 0; r < 2; ++r) acc[r][c] = mfma16(wf, __builtin_bit_cast(bf16x8, hf[r][s][m]), acc[r][c]);
      }
#pragma unroll
    for (int c = 0; c < 4; ++c) {
      const s16x4 wh = trread(wadr(f0 + 96, c));
#pragma unroll
      for (int r = 0; r < 2; ++r) acc[r][c] = __builtin_amdgcn_mfma_f32_16x16x16bf16_1k(wh, __builtin_bit_cast(s16x4, hh[r][s]), acc[r][c], 0, 0, 0);
    }
  }
  BAR();
  LAS f32x4* red = (LAS f32x4*)(lds + K3_W);
#pragma unroll
  for (int r = 0; r < 2; ++r)
#pragma unroll
    for (int c = 0; c < 4; ++c) red[(((wm * 2 + r) * 4 + c) * 4 + wk) * 64 + lane] = acc[r][c];
  BAR();
  {
    const int r = wk >> 1, cg = wk & 1, rt = 2 * wm + r;
    f32x4 o[2];
#pragma unroll
    for (int n = 0; n < 2; ++n) {
      o[n] = red[(((wm * 2 + r) * 4 + 2 * cg + n) * 4 + 0) * 64 + lane];
#pragma unroll
      for (int k2 = 1; k2 < 4; ++k2) o[n] += red[(((wm * 2 + r) * 4 + 2 * cg + n) * 4 + k2) * 64 + lane];
    }
    const int t = 64 * ib + 16 * rt + fr, d0 = 64 * jd + 32 * cg + 8 * g;
    const u32x4 xv = *(const u32x4*)(p.x + (size_t)t * 512 + d0);
    s16x4 ya[2];
#pragma unroll
    for (int n = 0; n < 2; ++n) {
      const int row = 32 * cg + 8 * g + 4 * n + (fr >> 2), col = 16 * rt + 4 * (fr & 3), chunk = col >> 3;
      ya[n] = trread(lds + K3_Y + row * 128 + ((chunk ^ (2 * ((row >> 1) & 3))) * 16) + (col & 4) * 2);
    }
    u32x4 res;
#pragma unroll
    for (int n = 0; n < 2; ++n)
#pragma unroll
      for (int e = 0; e < 2; ++e) {
        const unsigned xw = xv[2 * n + e];
        const float y0 = bf2f((unsigned)(u16)ya[n][2 * e]), y1 = bf2f((unsigned)(u16)ya[n][2 * e + 1]);
        res[2 * n + e] = pk2(rb(bflo(xw) + y0) + rb(o[n][2 * e]), rb(bfhi(xw) + y1) + rb(o[n][2 * e + 1]));
      }
    *(u32x4*)(p.out + (size_t)t * 512 + d0) = res;
  }
}

extern "C" void kernel_launch(void* const* d_in, const int* in_sizes, int n_in, void* d_out, int out_size, void* d_ws, size_t ws_size,
                              hipStream_t stream) {
  (void)in_sizes; (void)n_in; (void)out_size; (void)ws_size;
  Params p;
  p.x = (const u16*)d_in[0]; p.a_re = (const u16*)d_in[1]; p.a_im = (const u16*)d_in[2]; p.b_re = (const u16*)d_in[3];
  p.b_im = (const u16*)d_in[4]; p.c_re = (const u16*)d_in[5]; p.c_im = (const u16*)d_in[6]; p.log_dt = (const u16*)d_in[7];
  p.dskip = (const u16*)d_in[8]; p.w_in = (const u16*)d_in[10]; p.w_sp = (const u16*)d_in[11]; p.b_sp = (const u16*)d_in[12];
  p.w_out = (const u16*)d_in[13]; p.out = (u16*)d_out; p.ws = (char*)d_ws;
  (void)hipFuncSetAttribute((const void*)k1_kernel, hipFuncAttributeMaxDynamicSharedMemorySize, K1_LDS);
  (void)hipFuncSetAttribute((const void*)k2_kernel, hipFuncAttributeMaxDynamicSharedMemorySize, K2_LDS);
  (void)hipFuncSetAttribute((const void*)k3_kernel, hipFuncAttributeMaxDynamicSharedMemorySize, K3_LDS);
  (void)hipMemsetAsync((char*)d_ws + WS_XCH, 0, XCH_BYTES, stream);
  k1_kernel<<<256, 512, K1_LDS, stream>>>(p);
  k2_kernel<<<256, 512, K2_LDS, stream>>>(p);
  k3_kernel<<<256, 512, K3_LDS, stream>>>(p);
}
```

```cpp
#include <hip/hip_runtime.h>
#include <stdint.h>

typedef unsigned short u16;
typedef __bf16 bf16x8 __attribute__((ext_vector_type(8)));
typedef __bf16 bf16x2 __attribute__((ext_vector_type(2)));
typedef short s16x4 __attribute__((ext_vector_type(4)));
typedef short s16x8 __attribute__((ext_vector_type(8)));
typedef float f32x4 __attribute__((ext_vector_type(4)));
typedef float f32x2 __attribute__((ext_vector_type(2)));
typedef unsigned u32x4 __attribute__((ext_vector_type(4)));
typedef unsigned u32x2 __attribute__((ext_vector_type(2)));
#define LAS __attribute__((address_space(3)))
#define GAS __attribute__((address_space(1)))
typedef GAS unsigned long long gu64;
typedef GAS unsigned gu32;
#define RLX_AGENT __ATOMIC_RELAXED, __HIP_MEMORY_SCOPE_AGENT

constexpr size_t WS_XCH = 0;
constexpr size_t WS_EP = 512u << 10;
constexpr size_t WS_Z1F = 1u << 20;
constexpr size_t WS_Z2F = 5u << 20;
constexpr size_t WS_XNT = 9u << 20;
constexpr size_t WS_KG = 11u << 20;
constexpr size_t WS_HF = 14u << 20;
constexpr size_t WS_YA2 = 18u << 20;

struct Params {
  const u16* x; const u16* a_re; const u16* a_im; const u16* b_re; const u16* b_im; const u16* c_re; const u16* c_im;
  const u16* log_dt; const u16* dskip; const u16* w_in; const u16* w_sp; const u16* b_sp; const u16* w_out;
  u16* out; char* ws;
};

__device__ __forceinline__ float bf2f(unsigned h) { return __builtin_bit_cast(float, h << 16); }
__device__ __forceinline__ float bflo(unsigned w) { return __builtin_bit_cast(float, w << 16); }
__device__ __forceinline__ float bfhi(unsigned w) { return __builtin_bit_cast(float, w & 0xffff0000u); }
__device__ __forceinline__ unsigned pk2(float lo, float hi) { bf16x2 v = {(__bf16)lo, (__bf16)hi}; return __builtin_bit_cast(unsigned, v); }
__device__ __forceinline__ float rb(float v) { return (float)(__bf16)v; }
template <int CTRL> __device__ __forceinline__ float dpp_add(float v) {
  return v + __builtin_bit_cast(float, __builtin_amdgcn_update_dpp(0, __builtin_bit_cast(int, v), CTRL, 0xf, 0xf, true));
}
__device__ __forceinline__ float allred16(float v) { v = dpp_add<0xB1>(v); v = dpp_add<0x4E>(v); v = dpp_add<0x141>(v); v = dpp_add<0x140>(v); return v; }
__device__ __forceinline__ float gelu_tanh(float v) {
  const float t = v * v;
  const float u = v * __builtin_fmaf(t, 0.044715f, 1.0f);
  const float e = __builtin_amdgcn_exp2f(u * (-2.0f * 0.7978845608028654f * 1.4426950408889634f));
  return v * __builtin_amdgcn_rcpf(1.0f + e);
}
#define GLDS16(g, l) __builtin_amdgcn_global_load_lds((const unsigned*)(g), (LAS unsigned*)(l), 16, 0, 0)
#define GLDS4(g, l) __builtin_amdgcn_global_load_lds((const unsigned*)(g), (LAS unsigned*)(l), 4, 0, 0)
#define WAIT_V(n) asm volatile("s_waitcnt vmcnt(%0)" ::"i"(n) : "memory")
#define BAR() do { asm volatile("s_waitcnt lgkmcnt(0)" ::: "memory"); __builtin_amdgcn_s_barrier(); asm volatile("" ::: "memory"); } while (0)
__device__ __forceinline__ f32x4 mfma16(bf16x8 a, bf16x8 b, f32x4 c) { return __builtin_amdgcn_mfma_f32_16x16x32_bf16(a, b, c, 0, 0, 0); }
__device__ __forceinline__ s16x4 trread(LAS char* a) { return __builtin_amdgcn_ds_read_tr16_b64_v4i16((LAS s16x4*)a); }
#define CAS __attribute__((address_space(4)))
__device__ __forceinline__ unsigned ldsaddr(LAS char* a) { return (unsigned)(uintptr_t)a; }
#define TRREAD_ASM(dst, addr, off) asm volatile("ds_read_b64_tr_b16 %0, %1 offset:%2" : "=v"(dst) : "v"(addr), "i"(off) : "memory")
__device__ __forceinline__ bf16x8 cat8(s16x4 lo, s16x4 hi) { return __builtin_bit_cast(bf16x8, __builtin_shufflevector(lo, hi, 0, 1, 2, 3, 4, 5, 6, 7)); }

#define STAMP_DECL
#define STAMP(i)
#define STAMP_FLUSH(kern, n)

constexpr int K1_RING = 65536, K1_STG = 16384, K1_MISC = K1_RING + 5 * K1_STG  ;
constexpr int K1_ST = K1_MISC, K1_S1 = K1_ST + 1024, K1_P = K1_S1 + 512, K1_S2 = K1_P + 2048, K1_LDS = K1_S2 + 512;

__device__ __forceinline__ f32x2 gelu2(f32x2 v) {
  constexpr float C1 = -2.0f * 0.7978845608028654f * 1.4426950408889634f, C2 = C1 * 0.044715f;
  const f32x2 t = v * v;
  const f32x2 u = v * (t * C2 + C1);
  f32x2 e; e[0] = __builtin_amdgcn_exp2f(u[0]); e[1] = __builtin_amdgcn_exp2f(u[1]);
  e = e + 1.0f;
  f32x2 r; r[0] = __builtin_amdgcn_rcpf(e[0]); r[1] = __builtin_amdgcn_rcpf(e[1]);
  return v * r;
}
__device__ __forceinline__ f32x2 rb2(f32x2 v) { const unsigned u = pk2(v[0], v[1]); return (f32x2){bflo(u), bfhi(u)}; }

__global__ __launch_bounds__(512, 2) void k1_kernel(Params p) {
  extern __shared__ __attribute__((aligned(16))) char smem[];
  LAS char* lds = (LAS char*)smem;
  const int tid = threadIdx.x, lane = tid & 63, w = __builtin_amdgcn_readfirstlane(tid >> 6);
  const int b = blockIdx.x, xcd = b & 7, idx = b >> 3;
  const int fr = lane & 15, g = lane >> 4;
  if (idx >= 28) {
    const int sp = xcd * 4 + (idx - 28);
    const char* src = (const char*)p.w_sp + (size_t)(64 * sp + 8 * w) * 4096 + lane * 16;
#pragma unroll 4
    for (int n = 0; n < 32; ++n) GLDS16(src + n * 1024, lds + w * 8192 + (n & 7) * 1024);
    if (w < 7) {
      const char* so = (const char*)p.w_out + (size_t)(28 * sp + 4 * w) * 1024 + lane * 16;
#pragma unroll
      for (int n = 0; n < 4; ++n) GLDS16(so + n * 1024, lds + w * 8192 + n * 1024);
    }
    WAIT_V(0);
    return;
  }
  const int ib = 4 * xcd + (idx & 3), jb = idx >> 2;
  const int rot = ib & 7;
  const int wn = w & 3, wk = w >> 2;
  STAMP_DECL
  STAMP(0);
  const unsigned tag = *(const CAS unsigned*)(p.ws + WS_EP) + 1u;

  unsigned voffB[2];
#pragma unroll
  for (int nn = 0; nn < 2; ++nn) {
    const int n = 2 * w + nn, row = 4 * n + g, chunk = fr ^ ((g << 2) | (n & 3));
    voffB[nn] = (unsigned)(row * 1792) * 2u + (unsigned)chunk * 16u;
  }
  auto stageB = [&](int s) {
    const int half = s >> 3, kst = ((s & 7) + rot) & 7;
    const char* base = (const char*)p.w_in + ((size_t)(64 * kst) * 1792 + (half == 0 ? 896 : 0) + 128 * jb) * 2;
    LAS char* dst = lds + K1_RING + (s % 5) * K1_STG + w * 2048;
    GLDS16(base + voffB[0], dst);
    GLDS16(base + voffB[1], dst + 1024);
  };
#pragma unroll
  for (int n = 0; n < 8; ++n) {
    const int r = 8 * w + ((n + jb) & 7);
    GLDS16(p.x + (size_t)(64 * ib + r) * 512 + ((lane & 48) | ((lane & 15) ^ (r & 15))) * 8, lds + r * 1024);
  }
  stageB(0); stageB(1); stageB(2); stageB(3);
  WAIT_V(8);
  BAR();
  STAMP(1);
  bf16x8 af[4][8];
#pragma unroll
  for (int s = 0; s < 8; ++s) {
    const int chunk = 4 * (2 * ((s + rot) & 7) + wk) + g;
    const int pos = (chunk & ~15) | ((chunk & 15) ^ fr);
#pragma unroll
    for (int rt = 0; rt < 4; ++rt) af[rt][s] = *(LAS bf16x8*)(lds + (16 * rt + fr) * 1024 + pos * 16);
  }
  const bf16x8 ones = __builtin_bit_cast(bf16x8, (u32x4){0x3F803F80u, 0x3F803F80u, 0x3F803F80u, 0x3F803F80u});
  {
    f32x4 sa = {0, 0, 0, 0}, sq = {0, 0, 0, 0};
#pragma unroll
    for (int s = 0; s < 8; ++s) {
      const int chunk = 4 * (2 * s + wk) + g;
      const bf16x8 xf = *(LAS bf16x8*)(lds + (16 * wn + fr) * 1024 + ((chunk & ~15) | ((chunk & 15) ^ fr)) * 16);
      sa = mfma16(xf, ones, sa);
      sq = mfma16(xf, xf, sq);
    }
    LAS float* ST = (LAS float*)(lds + K1_ST);
    if (fr == 0) {
#pragma unroll
      for (int r = 0; r < 4; ++r) ST[(16 * wn + 4 * g + r) * 4 + 2 * wk] = sa[r];
    }
    const float dq = (fr & 2) ? ((fr & 1) ? sq[3] : sq[2]) : ((fr & 1) ? sq[1] : sq[0]);
    if ((fr >> 2) == g) ST[(16 * wn + fr) * 4 + 2 * wk + 1] = dq;
  }
  BAR();
  if (w == 0) {
    const f32x4 v = *(LAS f32x4*)(lds + K1_ST + lane * 16);
    const float mean = (v[0] + v[2]) * (1.0f / 512.0f);
    const float var = __builtin_fmaxf((v[1] + v[3]) * (1.0f / 512.0f) - mean * mean, 0.f);
    const float den = rb(__builtin_sqrtf(rb(rb(var) + 1e-5f)));
    *(LAS f32x2*)(lds + K1_S1 + lane * 8) = (f32x2){rb(mean), 1.0f / den};
  }
  BAR();
  STAMP(2);
  {
    const int nblk = jb < 4 ? 5 : 4;
    for (int u = w; u < 2 * nblk; u += 8) {
      const int dblk = jb + 7 * (u >> 1), t0 = 32 * (u & 1) + 8 * g;
      const int chunk = 2 * dblk + ((fr & 3) >> 1);
      s16x4 h[2];
#pragma unroll
      for (int tt = 0; tt < 2; ++tt) {
        const int row = t0 + 4 * tt + (fr >> 2);
        const unsigned ad = ldsaddr(lds + row * 1024 + ((chunk & ~15) | ((chunk & 15) ^ (row & 15))) * 16 + 8 * (fr & 1));
        TRREAD_ASM(h[tt], ad, 0);
      }
      f32x4 st[4];
#pragma unroll
      for (int e = 0; e < 4; ++e) st[e] = *(LAS f32x4*)(lds + K1_S1 + (t0 + 2 * e) * 8);
      asm volatile("s_waitcnt lgkmcnt(0)" : "+v"(h[0]), "+v"(h[1])::"memory");
      const u32x2 h0 = __builtin_bit_cast(u32x2, h[0]), h1 = __builtin_bit_cast(u32x2, h[1]);
      u32x4 o;
#pragma unroll
      for (int e = 0; e < 4; ++e) {
        const unsigned xw = e < 2 ? h0[e] : h1[e - 2];
        const f32x2 dv = rb2((f32x2){bflo(xw) - st[e][0], bfhi(xw) - st[e][2]});
        o[e] = pk2(dv[0] * st[e][1], dv[1] * st[e][3]);
      }
      *(u32x4*)(p.ws + WS_XNT + ((size_t)(16 * dblk + fr) * 2048 + 64 * ib + t0) * 2) = o;
    }
  }
  STAMP(3);

  unsigned boff[2][2];
#pragma unroll
  for (int c = 0; c < 2; ++c)
#pragma unroll
    for (int t = 0; t < 2; ++t) {
      const int row = 32 * wk + 8 * g + 4 * t + (fr >> 2), chn = 2 * (2 * wn + c) + ((fr & 3) >> 1);
      boff[c][t] = ldsaddr(lds + K1_RING) + 256 * row + 16 * (chn ^ (((fr >> 2) << 2) | ((2 * g + t) & 3))) + 8 * (fr & 1);
    }
  f32x4 acc[4][2], cs[2];
  f32x4 za[2][2], zcs[2];
  LAS f32x4* scr = (LAS f32x4*)lds;
  auto kexch_write = [&]() {
    if (wk) {
#pragma unroll
      for (int r = 0; r < 2; ++r)
#pragma unroll
        for (int c = 0; c < 2; ++c) { const f32x4 tmp = acc[r][c]; acc[r][c] = acc[2 + r][c]; acc[2 + r][c] = tmp; asm volatile("" : "+v"(acc[r][c]), "+v"(acc[2 + r][c])); }
    }
#pragma unroll
    for (int r = 0; r < 2; ++r)
#pragma unroll
      for (int c = 0; c < 2; ++c) scr[((wn * 2 + wk) * 6 + 2 * r + c) * 64 + lane] = acc[2 + r][c];
#pragma unroll
    for (int c = 0; c < 2; ++c) scr[((wn * 2 + wk) * 6 + 4 + c) * 64 + lane] = cs[c];
  };
#pragma unroll
  for (int s = 0; s < 16; ++s) {
    if (s == 0 || s == 8) {
#pragma unroll
      for (int rt = 0; rt < 4; ++rt)
#pragma unroll
        for (int c = 0; c < 2; ++c) acc[rt][c] = (f32x4){0, 0, 0, 0};
      cs[0] = (f32x4){0, 0, 0, 0}; cs[1] = (f32x4){0, 0, 0, 0};
    }
    if (s <= 12) WAIT_V(6); else if (s == 13) WAIT_V(4); else if (s == 14) WAIT_V(2); else WAIT_V(0);
    BAR();
    if (s == 8) {
#pragma unroll
      for (int r = 0; r < 2; ++r)
#pragma unroll
        for (int c = 0; c < 2; ++c) za[r][c] += scr[((wn * 2 + (wk ^ 1)) * 6 + 2 * r + c) * 64 + lane];
#pragma unroll
      for (int c = 0; c < 2; ++c) zcs[c] += scr[((wn * 2 + (wk ^ 1)) * 6 + 4 + c) * 64 + lane];
    }
    if (wk == 0 && s + 4 < 16) stageB(s + 4);
    {
      s16x4 tr[2][2];
#pragma unroll
      for (int c = 0; c < 2; ++c)
#pragma unroll
        for (int t = 0; t < 2; ++t) { const unsigned ad = boff[c][t] + (s % 5) * K1_STG; TRREAD_ASM(tr[c][t], ad, 0); }
      asm volatile("s_waitcnt lgkmcnt(0)" : "+v"(tr[0][0]), "+v"(tr[0][1]), "+v"(tr[1][0]), "+v"(tr[1][1])::"memory");
#pragma unroll
      for (int c = 0; c < 2; ++c) {
        const bf16x8 bf = cat8(tr[c][0], tr[c][1]);
#pragma unroll
        for (int rt = 0; rt < 4; ++rt) {
          if (s < 8) acc[rt][c] = mfma16(af[rt][s & 7], bf, acc[rt][c]);
          else acc[rt][c] = mfma16(bf, af[rt][s & 7], acc[rt][c]);
        }
        if (s < 8) cs[c] = mfma16(ones, bf, cs[c]); else cs[c] = mfma16(bf, ones, cs[c]);
      }
    }
    if (s >= 8) {
      const int rp = (s - 8) >> 2, r = (s - 8) & 3;
      const int row = 32 * wk + 16 * rp + 4 * g + r;
      const f32x2 st = *(LAS f32x2*)(lds + K1_S1 + row * 8);
      f32x2 v = {za[rp][0][r], za[rp][1][r]};
      v = (v - st[0] * (f32x2){zcs[0][0], zcs[1][0]}) * st[1];
      v = rb2(gelu2(rb2(v)));
      za[rp][0][r] = v[0]; za[rp][1][r] = v[1];
      const float mw = allred16(v[0] + v[1]) * (1.0f / 32.0f);
      const f32x2 dd = v - mw;
      const float q2 = allred16(dd[0] * dd[0] + dd[1] * dd[1]);
      if (fr == 0) *(LAS f32x2*)(lds + K1_P + (row * 4 + wn) * 8) = (f32x2){mw, q2};
    }
    if (wk == 1 && s + 4 < 16) stageB(s + 4);
    if (s == 7) {
      kexch_write();
#pragma unroll
      for (int r = 0; r < 2; ++r)
#pragma unroll
        for (int c = 0; c < 2; ++c) za[r][c] = acc[r][c];
      zcs[0] = cs[0]; zcs[1] = cs[1];
      STAMP(4);
    }
  }
  STAMP(5);
  kexch_write();
  BAR();
  if (w == 0) {
    const f32x4 a = *(LAS f32x4*)(lds + K1_P + lane * 32), c = *(LAS f32x4*)(lds + K1_P + lane * 32 + 16);
    const float mj = (a[0] + a[2] + c[0] + c[2]) * 0.25f;
    const float da = a[0] - mj, db = a[2] - mj, dc = c[0] - mj, dd = c[2] - mj;
    const float m2 = (a[1] + a[3]) + (c[1] + c[3]) + 32.0f * ((da * da + db * db) + (dc * dc + dd * dd));
    gu64* slot = (gu64*)(p.ws + WS_XCH) + ((size_t)(ib * 7 + jb) * 64 + lane) * 2;
    __hip_atomic_store(slot, ((unsigned long long)tag << 32) | __builtin_bit_cast(unsigned, mj), RLX_AGENT);
    __hip_atomic_store(slot + 1, ((unsigned long long)tag << 32) | __builtin_bit_cast(unsigned, m2), RLX_AGENT);
  }
  STAMP(6);
#pragma unroll
  for (int rp = 0; rp < 2; ++rp) {
    const f32x2 st = *(LAS f32x2*)(lds + K1_S1 + (32 * wk + 16 * rp + fr) * 8);
    u32x4 o;
#pragma unroll
    for (int c = 0; c < 2; ++c) {
      const f32x4 v = acc[rp][c] + scr[((wn * 2 + (wk ^ 1)) * 6 + 2 * rp + c) * 64 + lane];
      const f32x4 cc = cs[c] + scr[((wn * 2 + (wk ^ 1)) * 6 + 4 + c) * 64 + lane];
#pragma unroll
      for (int h = 0; h < 2; ++h) {
        f32x2 z = {v[2 * h], v[2 * h + 1]};
        z = (z - st[0] * (f32x2){cc[2 * h], cc[2 * h + 1]}) * st[1];
        z = gelu2(rb2(z));
        o[2 * c + h] = pk2(z[0], z[1]);
      }
    }
    const int t16 = 4 * ib + 2 * wk + rp, cp = 4 * jb + wn;
    *(u32x4*)(p.ws + WS_Z1F + ((size_t)(t16 * 28 + cp) * 64 + lane) * 16) = o;
  }
  STAMP(7);
  if (w == 0) {
    gu64* slot = (gu64*)(p.ws + WS_XCH) + ((size_t)(ib * 7) * 64 + lane) * 2;
    float mj[7], m2j[7];
    for (unsigned spins = 0; spins < (1u << 22); ++spins) {
      bool ok = true;
#pragma unroll
      for (int j = 0; j < 7; ++j) {
        const unsigned long long u0 = __hip_atomic_load(slot + j * 128, RLX_AGENT), u1 = __hip_atomic_load(slot + j * 128 + 1, RLX_AGENT);
        mj[j] = __builtin_bit_cast(float, (unsigned)u0); m2j[j] = __builtin_bit_cast(float, (unsigned)u1);
        ok &= ((unsigned)(u0 >> 32) == tag) & ((unsigned)(u1 >> 32) == tag);
      }
      if (__all(ok)) break;
      __builtin_amdgcn_s_sleep(1);
    }
    float mean = 0.f;
#pragma unroll
    for (int j = 0; j < 7; ++j) mean += mj[j];
    mean *= (1.0f / 7.0f);
    float m2 = 0.f;
#pragma unroll
    for (int j = 0; j < 7; ++j) { const float dm = mj[j] - mean; m2 += m2j[j] + 128.0f * dm * dm; }
    const float var = m2 * (1.0f / 896.0f);
    const float den = rb(__builtin_sqrtf(rb(rb(var) + 1e-5f)));
    *(LAS f32x2*)(lds + K1_S2 + lane * 8) = (f32x2){rb(mean), 1.0f / den};
  }
  BAR();
  STAMP(8);
  {
#pragma unroll
    for (int c = 0; c < 2; ++c) {
      u32x4 o;
#pragma unroll
      for (int rp = 0; rp < 2; ++rp) {
        const f32x4 s01 = *(LAS f32x4*)(lds + K1_S2 + (32 * wk + 16 * rp + 4 * g) * 8), s23 = *(LAS f32x4*)(lds + K1_S2 + (32 * wk + 16 * rp + 4 * g + 2) * 8);
        const f32x2 d0 = rb2((f32x2){za[rp][c][0] - s01[0], za[rp][c][1] - s01[2]}), d1 = rb2((f32x2){za[rp][c][2] - s23[0], za[rp][c][3] - s23[2]});
        o[2 * rp] = pk2(d0[0] * s01[1], d0[1] * s01[3]); o[2 * rp + 1] = pk2(d1[0] * s23[1], d1[1] * s23[3]);
      }
      const int ks = 2 * ib + wk, c16 = 8 * jb + 2 * wn + c;
      *(u32x4*)(p.ws + WS_Z2F + ((size_t)(ks * 56 + c16) * 64 + lane) * 16) = o;
    }
  }
  if (b == 0 && w == 0) {
    gu64* slot = (gu64*)(p.ws + WS_XCH);
    for (unsigned spins = 0; spins < (1u << 22); ++spins) {
      bool ok = true;
#pragma unroll
      for (int q = 0; q < 4; ++q) {
        const int ij = lane + 64 * q;
        if (ij < 224) ok &= ((unsigned)(__hip_atomic_load(slot + (size_t)ij * 128, RLX_AGENT) >> 32) == tag);
      }
      if (__all(ok)) break;
      __builtin_amdgcn_s_sleep(1);
    }
    if (lane == 0) __hip_atomic_store((gu32*)(p.ws + WS_EP), tag, RLX_AGENT);
  }
  STAMP(9);
  STAMP_FLUSH(0, 10);
}

constexpr int K2_A = 0, K2_TAB = 65536, K2_TABW = 64 * 272  , K2_RCS = 4160, K2_RCCH = 8 * K2_RCS  ,
              K2_U = K2_TAB + 4 * K2_TABW  , K2_G = K2_U + 2 * 4608  , K2_FLG = K2_G + 2 * 4128  ,
              K2_LDS = K2_FLG + 64;
constexpr int K2_SCR = K2_TAB + 2 * K2_RCCH  ;

__device__ __forceinline__ f32x2 cmul(f32x2 a, f32x2 b) {
  const f32x2 t = (f32x2){a[0], a[0]} * b;
  return (f32x2){a[1], a[1]} * (f32x2){-b[1], b[0]} + t;
}
__device__ __forceinline__ void side_sync(LAS char* lds, int sw, int step, int mask, int lane) {
  LAS int* flg = (LAS int*)(lds + K2_FLG);
  asm volatile("s_waitcnt lgkmcnt(0)" ::: "memory");
  if (lane == 0) __hip_atomic_store(flg + sw, step, __ATOMIC_RELAXED, __HIP_MEMORY_SCOPE_WORKGROUP);
  for (;;) {
    const u32x4 v = *(volatile LAS u32x4*)flg;
    bool ok = true;
#pragma unroll
    for (int i = 0; i < 4; ++i) ok &= (!((mask >> i) & 1)) || ((int)v[i] >= step);
    if (__builtin_amdgcn_readfirstlane(ok)) break;
    __builtin_amdgcn_s_sleep(1);
  }
  asm volatile("" ::: "memory");
}

__global__ __launch_bounds__(512, 2) void k2_kernel(Params p) {
  extern __shared__ __attribute__((aligned(16))) char smem[];
  LAS char* lds = (LAS char*)smem;
  const int tid = threadIdx.x, lane = tid & 63, w = __builtin_amdgcn_readfirstlane(tid >> 6);
  const int b = blockIdx.x, xcd = b & 7, idx = b >> 3;
  const int ib = (xcd >> 1) * 8 + (idx & 7), jb = (xcd & 1) * 4 + (idx >> 3);
#define K2ROT 0
  const int rot = K2ROT == 1 ? ((4 * (idx & 7) + 8 * (idx >> 3)) & 31) : K2ROT == 2 ? (((idx & 7) + 2 * (idx >> 3)) & 7) : 0;
  auto gmap = [&](int q) { return K2ROT == 2 ? ((q & ~7) | ((q + rot) & 7)) : ((q + rot) & 31); };
  const int fr = lane & 15, g = lane >> 4;
  STAMP_DECL
  STAMP(0);
  if (tid < 4) *(LAS int*)(lds + K2_FLG + tid * 4) = 0;
  BAR();
  f32x4 acc[4][7];
  if (w < 4) {
    const int cw = w;
#pragma unroll
    for (int r = 0; r < 4; ++r)
#pragma unroll
      for (int c = 0; c < 7; ++c) acc[r][c] = (f32x4){0, 0, 0, 0};
    const int r8 = lane >> 3;
    unsigned voffA[2];
#pragma unroll
    for (int par = 0; par < 2; ++par) voffA[par] = (unsigned)r8 * 4096u + (unsigned)(((lane & 7) ^ (((r8 >> 1) + 4 * par) & 7)) * 16);
    const char* gA = (const char*)p.w_sp + (size_t)(64 * ib) * 4096;
    const char* gB = p.ws + WS_Z2F + (size_t)(7 * jb + 3) * 1024;
    const unsigned lane16 = lane * 16;
    LAS char* abuf = lds + K2_A + cw * 16384;
    auto issueA = [&](int gl) {
      const int gi = gmap(4 * gl + cw);
      const char* base = gA + gi * 128;
#pragma unroll
      for (int n = 0; n < 8; ++n) GLDS16(base + n * 32768 + voffA[n & 1], abuf + (gl & 1) * 8192 + n * 1024);
    };
    u32x4 bq[3][7];
    auto issueB = [&](int s, u32x4 (&dst)[7]) {
      const int gi = gmap(4 * (s >> 1) + cw);
      const char* base = gB + (size_t)(2 * gi + (s & 1)) * (56 * 1024);
#pragma unroll
      for (int c = 0; c < 7; ++c)
        asm volatile("global_load_dwordx4 %0, %1, %2 offset:%3" : "=v"(dst[c]) : "v"(lane16), "s"(base), "i"((c - 3) * 1024) : "memory");
    };
    unsigned aoff[2];
#pragma unroll
    for (int pp = 0; pp < 2; ++pp) { aoff[pp] = 0; }
    unsigned afr[2][2];
#pragma unroll
    for (int kk = 0; kk < 2; ++kk)
#pragma unroll
      for (int pp = 0; pp < 2; ++pp) afr[kk][pp] = ldsaddr(abuf) + fr * 128 + (((4 * kk + 2 * pp + (g >> 1)) ^ ((fr >> 1) & 7)) * 16) + (g & 1) * 8;
    issueA(0); issueB(0, bq[0]); issueB(1, bq[1]); issueA(1); issueB(2, bq[2]);
#pragma unroll
    for (int s = 0; s < 16; ++s) {
      u32x4 (&bc)[7] = bq[s % 3];
      if (s <= 13) asm volatile("s_waitcnt vmcnt(22)" : "+v"(bc[0]), "+v"(bc[1]), "+v"(bc[2]), "+v"(bc[3]), "+v"(bc[4]), "+v"(bc[5]), "+v"(bc[6])::"memory");
      else if (s == 14) asm volatile("s_waitcnt vmcnt(7)" : "+v"(bc[0]), "+v"(bc[1]), "+v"(bc[2]), "+v"(bc[3]), "+v"(bc[4]), "+v"(bc[5]), "+v"(bc[6])::"memory");
      else asm volatile("s_waitcnt vmcnt(0)" : "+v"(bc[0]), "+v"(bc[1]), "+v"(bc[2]), "+v"(bc[3]), "+v"(bc[4]), "+v"(bc[5]), "+v"(bc[6])::"memory");
      bf16x8 a[4];
#pragma unroll
      for (int rt = 0; rt < 4; ++rt) {
        const unsigned o = ((s >> 1) & 1) * 8192 + rt * 2048;
        a[rt] = cat8(*(LAS s16x4*)(uintptr_t)(afr[s & 1][0] + o), *(LAS s16x4*)(uintptr_t)(afr[s & 1][1] + o));
      }
#pragma unroll
      for (int rt = 0; rt < 4; ++rt)
#pragma unroll
        for (int c = 0; c < 7; ++c) acc[rt][c] = mfma16(__builtin_bit_cast(bf16x8, bc[c]), a[rt], acc[rt][c]);
      __builtin_amdgcn_sched_barrier(0);
      if (s + 3 < 16) {
        if (((s + 3) & 1) == 0) issueA((s + 3) >> 1);
        issueB(s + 3, bc);
      }
    }
    STAMP(1);
  } else {
    const int sw = w - 4, ch = sw >> 1, hh = sw & 1, d = 2 * b + ch;
    const int pm = 3 << (2 * ch);
    const int n = 64 * hh + lane;
    const float are = bf2f(p.a_re[d * 128 + n]), aim = bf2f(p.a_im[d * 128 + n]), bre = bf2f(p.b_re[d * 128 + n]), bim = bf2f(p.b_im[d * 128 + n]),
                cre = bf2f(p.c_re[d * 128 + n]), cim = bf2f(p.c_im[d * 128 + n]);
    const u32x4 u0 = *(const u32x4*)(p.ws + WS_XNT + ((size_t)d * 2048 + 1024 * hh) * 2 + lane * 16);
    const u32x4 u1 = *(const u32x4*)(p.ws + WS_XNT + ((size_t)d * 2048 + 1024 * hh + 512) * 2 + lane * 16);
    const unsigned ld2 = *(const CAS unsigned*)(p.log_dt + 2 * b);
    const unsigned dk2 = *(const CAS unsigned*)(p.dskip + 2 * b);
    const float dt = rb(expf(ch ? bfhi(ld2) : bflo(ld2)));
    LAS char* tab = lds + K2_TAB + sw * K2_TABW;
    STAMP(1);
    {
      const float lr = dt * are * 1.4426950408889634f, fq = dt * aim * 0.15915494309189535f;
      const float dbr = dt * bre, dbi = dt * bim;
      const f32x2 W = {cre * dbr - cim * dbi, cre * dbi + cim * dbr};
      auto cx = [&](float a) {
        const float m = __builtin_amdgcn_exp2f(lr * a), ph = __builtin_amdgcn_fractf(fq * a);
        return (f32x2){m * __builtin_amdgcn_cosf(ph), m * __builtin_amdgcn_sinf(ph)};
      };
      auto put = [&](int row, f32x2 v) { *(LAS unsigned*)(tab + row * 272 + lane * 4) = pk2(v[0], v[1]); };
      const f32x2 E1 = cx(1.f), E8 = cx(8.f), E128 = cx(128.f);
      f32x2 P[8];
      P[0] = (f32x2){1.f, 0.f}; P[1] = E1;
#pragma unroll
      for (int r = 2; r < 8; ++r) P[r] = cmul(P[r - 1], E1);
#pragma unroll
      for (int r = 0; r < 8; ++r) { put(r, P[r]); put(8 + r, cmul(P[r], E128)); }
      f32x2 F = cmul(E128, E128);
#pragma unroll
      for (int m = 0; m < 2; ++m) {
#pragma unroll
        for (int r = 0; r < 4; ++r) put(16 + 4 * m + r, r == 0 ? F : cmul(F, P[2 * r]));
        F = cmul(F, E128);
      }
#pragma unroll
      for (int m = 0; m < 4; ++m) {
        put(24 + 2 * m, F); put(25 + 2 * m, cmul(F, P[4]));
        F = cmul(F, E128);
      }
#pragma unroll
      for (int m = 0; m < 9; ++m) {
        put(32 + m, F);
        if (m < 8) F = cmul(F, E128);
      }
      f32x2 Y = W;
#pragma unroll
      for (int c = 0; c < 16; ++c) {
        put(48 + c, (f32x2){Y[0], -Y[1]});
        if (c < 15) Y = cmul(Y, E8);
      }
    }
    STAMP(2);
    *(LAS u32x4*)(lds + K2_U + ch * 4608 + 512 + hh * 2048 + lane * 16) = u0;
    *(LAS u32x4*)(lds + K2_U + ch * 4608 + 512 + hh * 2048 + 1024 + lane * 16) = u1;
    if (hh == 0 && lane < 32) *(LAS u32x4*)(lds + K2_U + ch * 4608 + lane * 16) = (u32x4){0, 0, 0, 0};
    f32x4 ka[3] = {{0, 0, 0, 0}, {0, 0, 0, 0}, {0, 0, 0, 0}};
#pragma unroll
    for (int ks = 0; ks < 4; ++ks) {
      const bf16x8 yf = *(LAS bf16x8*)(tab + (48 + fr) * 272 + ks * 64 + g * 16);
#pragma unroll
      for (int t = 0; t < 3; ++t) ka[t] = mfma16(*(LAS bf16x8*)(tab + (16 * t + fr) * 272 + ks * 64 + g * 16), yf, ka[t]);
    }
    if (hh == 1) {
#pragma unroll
      for (int t = 0; t < 3; ++t) *(LAS f32x4*)(tab + (t * 64 + lane) * 16) = ka[t];
    }
    STAMP(3);
    side_sync(lds, sw, 1, pm, lane);
    STAMP(4);
    LAS u16* T = (LAS u16*)(lds + K2_TAB + (2 * ch) * K2_TABW);
    if (hh == 0) {
      LAS char* oth = tab + K2_TABW;
#pragma unroll
      for (int t = 0; t < 3; ++t) {
        const f32x4 s = ka[t] + *(LAS f32x4*)(oth + (t * 64 + lane) * 16);
#pragma unroll
        for (int r = 0; r < 4; ++r) {
          const int row = 4 * g + r;
          const int a = t == 0 ? (row & 7) + 128 * (row >> 3)
                      : t == 1 ? (row < 8 ? 256 + 2 * (row & 3) + 128 * (row >> 2) : 512 + 4 * (row & 1) + 128 * ((row - 8) >> 1))
                               : (row < 8 ? 1024 + 128 * row : 2048);
          if (t < 2 || row <= 8) T[a + 8 * fr] = (u16)(pk2(s[r], 0.f) & 0xffffu);
        }
      }
    }
    side_sync(lds, sw, 2, pm, lane);
    STAMP(5);
    LAS char* G = lds + K2_G + ch * 4128;
#pragma unroll
    for (int it = 0; it < 2; ++it) {
      const int gc = 128 * hh + 64 * it + lane;
      u32x4 o;
#pragma unroll
      for (int e = 0; e < 8; e += 2) {
        const int l0 = 2047 - (8 * gc + e), l1 = l0 - 1;
        const unsigned cv = pk2((float)l0, (float)l1);
        const int i0 = (int)bflo(cv), i1 = (int)bfhi(cv);
        o[e >> 1] = (unsigned)T[i0] | ((unsigned)T[i1] << 16);
      }
      *(LAS u32x4*)(G + gc * 16) = o;
    }
    if (hh == 0 && lane == 0) *(LAS u32x4*)(G + 4096) = (u32x4){0, 0, 0, 0};
    side_sync(lds, sw, 3, 15, lane);
    STAMP(6);
    LAS char* rc = lds + K2_TAB + ch * K2_RCCH;
#pragma unroll
    for (int it = 0; it < 2; ++it) {
      const int gc = 128 * hh + 64 * it + lane;
      const u32x4 g0 = *(LAS u32x4*)(G + gc * 16), g1 = *(LAS u32x4*)(G + gc * 16 + 16);
      const unsigned wd[8] = {g0[0], g0[1], g0[2], g0[3], g1[0], g1[1], g1[2], g1[3]};
#pragma unroll
      for (int c = 0; c < 8; ++c) {
        const int s = 7 - c;
        u32x4 o;
#pragma unroll
        for (int i = 0; i < 4; ++i) o[i] = (s & 1) ? __builtin_amdgcn_alignbit(wd[(s >> 1) + i + 1], wd[(s >> 1) + i], 16) : wd[(s >> 1) + i];
        *(LAS u32x4*)(rc + c * K2_RCS + gc * 16) = o;
      }
    }
    if (hh == 0 && lane < 8) *(LAS u32x4*)(rc + lane * K2_RCS + 4096) = (u32x4){0, 0, 0, 0};
    side_sync(lds, sw, 4, pm, lane);
    STAMP(7);
    f32x4 cacc[8];
#pragma unroll
    for (int r = 0; r < 8; ++r) cacc[r] = (f32x4){0, 0, 0, 0};
    LAS char* ub = lds + K2_U + ch * 4608 + (240 + 16 * fr + 8 * g) * 2;
    LAS char* kb = rc + (fr & 7) * K2_RCS + (2024 + 8 * g - 8 * (fr >> 3)) * 2;
#pragma unroll 1
    for (int pp = 0; pp < 4; ++pp) {
      const int p0 = 4 * hh + pp;
      bf16x8 ua[8];
#pragma unroll
      for (int r = 0; r < 8; ++r) ua[r] = *(LAS bf16x8*)(ub + (8 * r - p0) * 64);
#pragma unroll
      for (int i = 0; i < 8; ++i) {
        const bf16x8 kf = *(LAS bf16x8*)(kb - (p0 + 8 * i) * 64);
#pragma unroll
        for (int rt = i; rt < 8; ++rt) cacc[rt] = mfma16(ua[rt - i], kf, cacc[rt]);
      }
    }
    STAMP(8);
    side_sync(lds, sw, 5, 15, lane);
    STAMP(9);
    {
      LAS float* part = (LAS float*)(lds + K2_TAB + sw * 8192);
#pragma unroll
      for (int rt = 0; rt < 8; ++rt)
#pragma unroll
        for (int r = 0; r < 4; ++r) part[256 * rt + 64 * g + 16 * r + fr] = cacc[rt][r];
    }
    side_sync(lds, sw, 6, 15, lane);
    STAMP(10);
    {
      const int tt = sw * 64 + lane;
      unsigned yb[2][4];
#pragma unroll
      for (int c2 = 0; c2 < 2; ++c2) {
        LAS f32x4* q0 = (LAS f32x4*)(lds + K2_TAB + (2 * c2) * 8192 + tt * 32);
        LAS f32x4* q1 = (LAS f32x4*)(lds + K2_TAB + (2 * c2 + 1) * 8192 + tt * 32);
        const f32x4 s0 = q0[0] + q1[0], s1 = q0[1] + q1[1];
        const u32x4 uv = *(LAS u32x4*)(lds + K2_U + c2 * 4608 + 512 + tt * 16);
        const float dsk = c2 ? bfhi(dk2) : bflo(dk2);
#pragma unroll
        for (int e = 0; e < 4; ++e) {
          const float y0 = e < 2 ? s0[2 * e] : s1[2 * e - 4], y1 = e < 2 ? s0[2 * e + 1] : s1[2 * e - 3];
          yb[c2][e] = pk2(rb(y0) + rb(bflo(uv[e]) * dsk), rb(y1) + rb(bfhi(uv[e]) * dsk));
        }
      }
      u32x4 o0, o1;
#pragma unroll
      for (int e = 0; e < 2; ++e) {
        o0[2 * e] = (yb[0][e] & 0xffffu) | (yb[1][e] << 16); o0[2 * e + 1] = (yb[0][e] >> 16) | (yb[1][e] & 0xffff0000u);
        o1[2 * e] = (yb[0][2 + e] & 0xffffu) | (yb[1][2 + e] << 16); o1[2 * e + 1] = (yb[0][2 + e] >> 16) | (yb[1][2 + e] & 0xffff0000u);
      }
      *(u32x4*)(p.ws + WS_YA2 + (size_t)b * 8192 + tt * 32) = o0;
      *(u32x4*)(p.ws + WS_YA2 + (size_t)b * 8192 + tt * 32 + 16) = o1;
    }
    STAMP(11);
  }
  BAR();
  STAMP(12);
  if (w < 4) {
    LAS f32x4* red = (LAS f32x4*)lds;
#pragma unroll
    for (int rt = 0; rt < 4; ++rt)
#pragma unroll
      for (int c = 0; c < 7; ++c)
        if (rt != w) red[((rt * 4 + w) * 7 + c) * 64 + lane] = acc[rt][c];
  }
  BAR();
  if (w < 4) {
    const int rt = w;
    LAS f32x4* red = (LAS f32x4*)lds;
    f32x4 mine[7];
#pragma unroll
    for (int c = 0; c < 7; ++c) mine[c] = acc[0][c];
    if (rt == 1) {
#pragma unroll
      for (int c = 0; c < 7; ++c) { mine[c] = acc[1][c]; asm volatile("" : "+v"(mine[c])); }
    } else if (rt == 2) {
#pragma unroll
      for (int c = 0; c < 7; ++c) { mine[c] = acc[2][c]; asm volatile("" : "+v"(mine[c])); }
    } else if (rt == 3) {
#pragma unroll
      for (int c = 0; c < 7; ++c) { mine[c] = acc[3][c]; asm volatile("" : "+v"(mine[c])); }
    }
    const int t = 64 * ib + 16 * rt + fr;
    const float bs = bf2f(p.b_sp[t]);
    const int t16 = 4 * ib + rt;
    unsigned hw[7][2];
#pragma unroll
    for (int c = 0; c < 7; ++c) {
      f32x4 v = mine[c];
#pragma unroll
      for (int k2 = 0; k2 < 4; ++k2)
        if (k2 != rt) v += red[((rt * 4 + k2) * 7 + c) * 64 + lane];
      const int c16 = 7 * jb + c;
      const u32x2 z = *(const u32x2*)(p.ws + WS_Z1F + ((size_t)(t16 * 28 + (c16 >> 1)) * 64 + lane) * 16 + (c16 & 1) * 8);
      const float h0 = bflo(z[0]) * rb(rb(v[0]) + bs), h1 = bfhi(z[0]) * rb(rb(v[1]) + bs);
      const float h2 = bflo(z[1]) * rb(rb(v[2]) + bs), h3 = bfhi(z[1]) * rb(rb(v[3]) + bs);
      hw[c][0] = pk2(h0, h1); hw[c][1] = pk2(h2, h3);
    }
    char* hb = p.ws + WS_HF + ((size_t)(ib * 8 + jb) * 4 + rt) * 3584;
#pragma unroll
    for (int m = 0; m < 3; ++m) *(u32x4*)(hb + m * 1024 + lane * 16) = (u32x4){hw[2 * m][0], hw[2 * m][1], hw[2 * m + 1][0], hw[2 * m + 1][1]};
    *(u32x2*)(hb + 3072 + lane * 8) = (u32x2){hw[6][0], hw[6][1]};
  }
  STAMP(13);
}

constexpr int K3_W = 0, K3_Y = 896 * 128  , K3_LDS = K3_Y;

__global__ __launch_bounds__(512, 2) void k3_kernel(Params p) {
  extern __shared__ __attribute__((aligned(16))) char smem[];
  LAS char* lds = (LAS char*)smem;
  const int tid = threadIdx.x, lane = tid & 63, w = __builtin_amdgcn_readfirstlane(tid >> 6);
  const int b = blockIdx.x, xcd = b & 7, idx = b >> 3;
  const int ib = (xcd >> 1) * 8 + (idx & 7), jd = (xcd & 1) * 4 + (idx >> 3);
  const int fr = lane & 15, g = lane >> 4;
  const int wm = w & 1, wk = w >> 1;
  STAMP_DECL
  STAMP(0);
  {
    const int r8 = lane >> 3;
    const char* src = (const char*)p.w_out + (size_t)r8 * 1024 + jd * 128 + (((lane & 7) ^ (2 * ((r8 >> 1) & 3))) * 16);
#pragma unroll
    for (int n = 0; n < 14; ++n) { const int pc = w * 14 + n; GLDS16(src + (size_t)pc * 8192, lds + K3_W + pc * 1024); }
  }
  u32x4 hf[2][2][3]; u32x2 hh[2][2];
#pragma unroll
  for (int r = 0; r < 2; ++r)
#pragma unroll
    for (int s = 0; s < 2; ++s) {
      const char* hb = p.ws + WS_HF + ((size_t)(ib * 8 + 2 * wk + s) * 4 + 2 * wm + r) * 3584;
#pragma unroll
      for (int m = 0; m < 3; ++m) hf[r][s][m] = *(const u32x4*)(hb + m * 1024 + lane * 16);
      hh[r][s] = *(const u32x2*)(hb + 3072 + lane * 8);
    }
  WAIT_V(0);
  BAR();
  STAMP(1);
  f32x4 acc[2][4];
#pragma unroll
  for (int r = 0; r < 2; ++r)
#pragma unroll
    for (int c = 0; c < 4; ++c) acc[r][c] = (f32x4){0, 0, 0, 0};
  auto wadr = [&](int frow, int c) {
    const int row = frow + 4 * g + (fr >> 2);
    const int col = 32 * (c >> 1) + 8 * (fr & 3) + 4 * (c & 1);
    const int chunk = col >> 3;
    return lds + K3_W + row * 128 + ((chunk ^ (2 * ((row >> 1) & 3))) * 16) + (col & 4) * 2;
  };
#pragma unroll
  for (int s = 0; s < 2; ++s) {
    const int f0 = 112 * (2 * wk + s);
#pragma unroll
    for (int m = 0; m < 3; ++m)
#pragma unroll
      for (int c = 0; c < 4; ++c) {
        const bf16x8 wf = cat8(trread(wadr(f0 + 32 * m, c)), trread(wadr(f0 + 32 * m + 16, c)));
#pragma unroll
        for (int r = 0; r < 2; ++r) acc[r][c] = mfma16(wf, __builtin_bit_cast(bf16x8, hf[r][s][m]), acc[r][c]);
      }
#pragma unroll
    for (int c = 0; c < 4; ++c) {
      const s16x4 wh = trread(wadr(f0 + 96, c));
#pragma unroll
      for (int r = 0; r < 2; ++r) acc[r][c] = __builtin_amdgcn_mfma_f32_16x16x16bf16_1k(wh, __builtin_bit_cast(s16x4, hh[r][s]), acc[r][c], 0, 0, 0);
    }
  }
  BAR();
  STAMP(2);
  LAS f32x4* red = (LAS f32x4*)(lds + K3_W);
#pragma unroll
  for (int r = 0; r < 2; ++r)
#pragma unroll
    for (int c = 0; c < 4; ++c) red[(((wm * 2 + r) * 4 + c) * 4 + wk) * 64 + lane] = acc[r][c];
  BAR();
  {
    const int r = wk >> 1, cg = wk & 1, rt = 2 * wm + r;
    f32x4 o[2];
#pragma unroll
    for (int n = 0; n < 2; ++n) {
      o[n] = red[(((wm * 2 + r) * 4 + 2 * cg + n) * 4 + 0) * 64 + lane];
#pragma unroll
      for (int k2 = 1; k2 < 4; ++k2) o[n] += red[(((wm * 2 + r) * 4 + 2 * cg + n) * 4 + k2) * 64 + lane];
    }
    const int t = 64 * ib + 16 * rt + fr, d0 = 64 * jd + 32 * cg + 8 * g;
    const u32x4 xv = *(const u32x4*)(p.x + (size_t)t * 512 + d0);
    unsigned ya[4];
#pragma unroll
    for (int e = 0; e < 4; ++e) ya[e] = *(const unsigned*)(p.ws + WS_YA2 + ((size_t)(d0 / 2 + e) * 2048 + t) * 4);
    u32x4 res;
#pragma unroll
    for (int n = 0; n < 2; ++n)
#pragma unroll
      for (int e = 0; e < 2; ++e) {
        const unsigned xw = xv[2 * n + e];
        const float y0 = bflo(ya[2 * n + e]), y1 = bfhi(ya[2 * n + e]);
        res[2 * n + e] = pk2(rb(bflo(xw) + y0) + rb(o[n][2 * e]), rb(bfhi(xw) + y1) + rb(o[n][2 * e + 1]));
      }
    *(u32x4*)(p.out + (size_t)t * 512 + d0) = res;
  }
  STAMP(3);
  STAMP_FLUSH(2, 4);
}

extern "C" void kernel_launch(void* const* d_in, const int* in_sizes, int n_in, void* d_out, int out_size, void* d_ws, size_t ws_size,
                              hipStream_t stream) {
  (void)in_sizes; (void)n_in; (void)out_size; (void)ws_size;
  Params p;
  p.x = (const u16*)d_in[0]; p.a_re = (const u16*)d_in[1]; p.a_im = (const u16*)d_in[2]; p.b_re = (const u16*)d_in[3];
  p.b_im = (const u16*)d_in[4]; p.c_re = (const u16*)d_in[5]; p.c_im = (const u16*)d_in[6]; p.log_dt = (const u16*)d_in[7];
  p.dskip = (const u16*)d_in[8]; p.w_in = (const u16*)d_in[10]; p.w_sp = (const u16*)d_in[11]; p.b_sp = (const u16*)d_in[12];
  p.w_out = (const u16*)d_in[13]; p.out = (u16*)d_out; p.ws = (char*)d_ws;
  (void)hipFuncSetAttribute((const void*)k1_kernel, hipFuncAttributeMaxDynamicSharedMemorySize, K1_LDS);
  (void)hipFuncSetAttribute((const void*)k2_kernel, hipFuncAttributeMaxDynamicSharedMemorySize, K2_LDS);
  (void)hipFuncSetAttribute((const void*)k3_kernel, hipFuncAttributeMaxDynamicSharedMemorySize, K3_LDS);
  k1_kernel<<<256, 512, K1_LDS, stream>>>(p);
  k2_kernel<<<256, 512, K2_LDS, stream>>>(p);
  k3_kernel<<<256, 512, K3_LDS, stream>>>(p);
}
```

```cpp
#include <hip/hip_runtime.h>
#include <stdint.h>

typedef unsigned short u16;
typedef __bf16 bf16x8 __attribute__((ext_vector_type(8)));
typedef __bf16 bf16x2 __attribute__((ext_vector_type(2)));
typedef short s16x4 __attribute__((ext_vector_type(4)));
typedef short s16x8 __attribute__((ext_vector_type(8)));
typedef float f32x4 __attribute__((ext_vector_type(4)));
typedef float f32x2 __attribute__((ext_vector_type(2)));
typedef unsigned u32x4 __attribute__((ext_vector_type(4)));
typedef unsigned u32x2 __attribute__((ext_vector_type(2)));
#define LAS __attribute__((address_space(3)))
#define GAS __attribute__((address_space(1)))
typedef GAS unsigned long long gu64;
typedef GAS unsigned gu32;
#define RLX_AGENT __ATOMIC_RELAXED, __HIP_MEMORY_SCOPE_AGENT

constexpr size_t WS_XCH = 0;
constexpr size_t WS_EP = 512u << 10;
constexpr size_t WS_CNT = 576u << 10;
constexpr size_t WS_Z1F = 1u << 20;
constexpr size_t WS_Z2F = 5u << 20;
constexpr size_t WS_XNT = 9u << 20;
constexpr size_t WS_KG = 11u << 20;
constexpr size_t WS_HF = 14u << 20;
constexpr size_t WS_YA2 = 18u << 20;

struct Params {
  const u16* x; const u16* a_re; const u16* a_im; const u16* b_re; const u16* b_im; const u16* c_re; const u16* c_im;
  const u16* log_dt; const u16* dskip; const u16* w_in; const u16* w_sp; const u16* b_sp; const u16* w_out;
  u16* out; char* ws;
};

__device__ __forceinline__ float bf2f(unsigned h) { return __builtin_bit_cast(float, h << 16); }
__device__ __forceinline__ float bflo(unsigned w) { return __builtin_bit_cast(float, w << 16); }
__device__ __forceinline__ float bfhi(unsigned w) { return __builtin_bit_cast(float, w & 0xffff0000u); }
__device__ __forceinline__ unsigned pk2(float lo, float hi) { bf16x2 v = {(__bf16)lo, (__bf16)hi}; return __builtin_bit_cast(unsigned, v); }
__device__ __forceinline__ float rb(float v) { return (float)(__bf16)v; }
template <int CTRL> __device__ __forceinline__ float dpp_add(float v) {
  return v + __builtin_bit_cast(float, __builtin_amdgcn_update_dpp(0, __builtin_bit_cast(int, v), CTRL, 0xf, 0xf, true));
}
__device__ __forceinline__ float allred16(float v) { v = dpp_add<0xB1>(v); v = dpp_add<0x4E>(v); v = dpp_add<0x141>(v); v = dpp_add<0x140>(v); return v; }
__device__ __forceinline__ float gelu_tanh(float v) {
  const float t = v * v;
  const float u = v * __builtin_fmaf(t, 0.044715f, 1.0f);
  const float e = __builtin_amdgcn_exp2f(u * (-2.0f * 0.7978845608028654f * 1.4426950408889634f));
  return v * __builtin_amdgcn_rcpf(1.0f + e);
}
#define GLDS16(g, l) __builtin_amdgcn_global_load_lds((const unsigned*)(g), (LAS unsigned*)(l), 16, 0, 0)
#define GLDS4(g, l) __builtin_amdgcn_global_load_lds((const unsigned*)(g), (LAS unsigned*)(l), 4, 0, 0)
#define WAIT_V(n) asm volatile("s_waitcnt vmcnt(%0)" ::"i"(n) : "memory")
#define BAR() do { asm volatile("s_waitcnt lgkmcnt(0)" ::: "memory"); __builtin_amdgcn_s_barrier(); asm volatile("" ::: "memory"); } while (0)
__device__ __forceinline__ f32x4 mfma16(bf16x8 a, bf16x8 b, f32x4 c) { return __builtin_amdgcn_mfma_f32_16x16x32_bf16(a, b, c, 0, 0, 0); }
__device__ __forceinline__ s16x4 trread(LAS char* a) { return __builtin_amdgcn_ds_read_tr16_b64_v4i16((LAS s16x4*)a); }
#define CAS __attribute__((address_space(4)))
__device__ __forceinline__ unsigned ldsaddr(LAS char* a) { return (unsigned)(uintptr_t)a; }
#define GLOAD16(dst, voff, sbase, off) asm volatile("global_load_dwordx4 %0, %1, %2 offset:%3" : "=v"(dst) : "v"(voff), "s"(sbase), "i"(off) : "memory")
#define TRREAD_ASM(dst, addr, off) asm volatile("ds_read_b64_tr_b16 %0, %1 offset:%2" : "=v"(dst) : "v"(addr), "i"(off) : "memory")
__device__ __forceinline__ bf16x8 cat8(s16x4 lo, s16x4 hi) { return __builtin_bit_cast(bf16x8, __builtin_shufflevector(lo, hi, 0, 1, 2, 3, 4, 5, 6, 7)); }

__device__ __forceinline__ const char* text_begin(unsigned& bytes) {
  unsigned long long pc; unsigned off, len;
  asm volatile("s_getpc_b64 %0\n.Lpc%=:\n\ts_mov_b32 %1, .Lpc%=-k1_kernel\n\ts_mov_b32 %2, zz_text_end-k1_kernel" : "=s"(pc), "=s"(off), "=s"(len));
  pc -= off;
  bytes = len;
  return (const char*)pc;
}

#define STAMP_DECL
#define STAMP(i)
#define STAMP_FLUSH(kern, n)

constexpr int K1_RING = 65536, K1_STG = 16384, K1_SCR = K1_RING + 3 * K1_STG  , K1_SCS = K1_SCR + 32768  ,
              K1_ST = K1_SCS + 1024, K1_S1 = K1_ST + 1024, K1_P = K1_S1 + 512, K1_S2 = K1_P + 2048, K1_PF = K1_S2 + 512, K1_LDS = K1_PF + 1024;

__device__ __forceinline__ f32x2 gelu2(f32x2 v) {
  constexpr float C1 = -2.0f * 0.7978845608028654f * 1.4426950408889634f, C2 = C1 * 0.044715f;
  const f32x2 t = v * v;
  const f32x2 u = v * (t * C2 + C1);
  f32x2 e; e[0] = __builtin_amdgcn_exp2f(u[0]); e[1] = __builtin_amdgcn_exp2f(u[1]);
  e = e + 1.0f;
  f32x2 r; r[0] = __builtin_amdgcn_rcpf(e[0]); r[1] = __builtin_amdgcn_rcpf(e[1]);
  return v * r;
}
__device__ __forceinline__ f32x2 rb2(f32x2 v) { const unsigned u = pk2(v[0], v[1]); return (f32x2){bflo(u), bfhi(u)}; }

extern "C" __global__ __launch_bounds__(768, 3) void k1_kernel(Params p) {
  extern __shared__ __attribute__((aligned(16))) char smem[];
  LAS char* lds = (LAS char*)smem;
  const int tid = threadIdx.x, lane = tid & 63, w = __builtin_amdgcn_readfirstlane(tid >> 6);
  const int b = blockIdx.x, xcd = b & 7, idx = b >> 3;
  const int fr = lane & 15, g = lane >> 4;
  if (idx >= 28) {
    if (w >= 8) return;
    const int sp = xcd * 4 + (idx - 28);
    const char* src = (const char*)p.w_sp + (size_t)(64 * sp + 8 * w) * 4096 + lane * 16;
#pragma unroll 4
    for (int n = 0; n < 32; ++n) GLDS16(src + n * 1024, lds + w * 8192 + (n & 7) * 1024);
    if (w < 7) {
      const char* so = (const char*)p.w_out + (size_t)(28 * sp + 4 * w) * 1024 + lane * 16;
#pragma unroll
      for (int n = 0; n < 4; ++n) GLDS16(so + n * 1024, lds + w * 8192 + n * 1024);
    }
    WAIT_V(0);
    return;
  }
  const int ib = 4 * xcd + (idx & 3), jb = idx >> 2;
  const int rot = ib & 7;
  STAMP_DECL
  STAMP(0);

  if (w >= 8) {
    const int lw = w - 8;
    unsigned tbytes;
    const char* cbase = text_begin(tbytes);
    if (lw == 3 && (unsigned)(idx + 1) * 1024u <= tbytes) GLDS16(cbase + idx * 1024 + lane * 16, lds + K1_PF);
    unsigned voff[4], xvoff[2];
#pragma unroll
    for (int nn = 0; nn < 4; ++nn) {
      const int row = 4 * (4 * lw + nn) + g;
      voff[nn] = (unsigned)(row * 1792) * 2u + (unsigned)(fr ^ ((g << 2) | nn)) * 16u;
    }
#pragma unroll
    for (int nn = 0; nn < 2; ++nn) {
      const int row = 16 * lw + 8 * nn + (lane >> 3);
      xvoff[nn] = (unsigned)row * 1024u + (unsigned)((lane & 7) ^ ((row >> 1) & 7)) * 16u;
    }
    auto ldS = [&](int s) {
      const int half = s >> 3, kst = ((s & 7) + rot) & 7;
      if (s < 8) {
        const char* xb = (const char*)p.x + (size_t)(64 * ib) * 1024 + kst * 128;
        LAS char* xd = lds + kst * 8192 + lw * 2048;
        GLDS16(xb + xvoff[0], xd); GLDS16(xb + xvoff[1], xd + 1024);
      }
      const char* base = (const char*)p.w_in + ((size_t)(64 * kst) * 1792 + (half == 0 ? 896 : 0) + 128 * jb) * 2;
      LAS char* dst = lds + K1_RING + (s % 3) * K1_STG + lw * 4096;
#pragma unroll
      for (int nn = 0; nn < 4; ++nn) GLDS16(base + voff[nn], dst + nn * 1024);
    };
    ldS(0); ldS(1);
#pragma unroll
    for (int s = 0; s < 16; ++s) {
      if (s + 1 < 8) WAIT_V(6); else if (s + 1 < 16) WAIT_V(4); else WAIT_V(0);
      BAR();
      if (s + 2 < 16) ldS(s + 2);
    }
    if (lw == 3) {
      for (unsigned o = (unsigned)(28 + idx) * 1024u; o + 1024u <= tbytes; o += 28u * 1024u) GLDS16(cbase + o + lane * 16, lds + K1_PF);
      WAIT_V(0);
    }
    BAR(); BAR(); BAR();
    return;
  }

  const int wn = w & 3, wk = w >> 2;
  const unsigned tag = *(const CAS unsigned*)(p.ws + WS_EP) + 1u;
  const bf16x8 ones = __builtin_bit_cast(bf16x8, (u32x4){0x3F803F80u, 0x3F803F80u, 0x3F803F80u, 0x3F803F80u});
  unsigned boff[2][2];
#pragma unroll
  for (int c = 0; c < 2; ++c)
#pragma unroll
    for (int t = 0; t < 2; ++t) {
      const int row = 32 * wk + 8 * g + 4 * t + (fr >> 2), chn = 2 * (2 * wn + c) + ((fr & 3) >> 1);
      boff[c][t] = K1_RING + 256 * row + 16 * (chn ^ (((fr >> 2) << 2) | ((2 * g + t) & 3))) + 8 * (fr & 1);
    }
  const unsigned aoff = (unsigned)(fr * 128 + (((4 * wk + g) ^ ((fr >> 1) & 7)) * 16));
  f32x4 acc[4][2], cs[2];
  f32x4 za[2][2];
  float zcs[2];
  f32x4 sa = {0, 0, 0, 0}, sq = {0, 0, 0, 0};
  LAS f32x4* scr = (LAS f32x4*)(lds + K1_SCR);
  LAS float* scs = (LAS float*)(lds + K1_SCS);
  struct Frag { bf16x8 a[4]; bf16x8 b[2]; bf16x8 sf; };
  Frag F0, F1;
  auto rd = [&](int s, Frag& F, bool stats) {
    LAS char* xa = lds + (((s & 7) + rot) & 7) * 8192 + aoff;
#pragma unroll
    for (int rt = 0; rt < 4; ++rt) F.a[rt] = *(LAS bf16x8*)(xa + rt * 2048);
    const int bo = (s % 3) * K1_STG;
#pragma unroll
    for (int c = 0; c < 2; ++c) F.b[c] = cat8(trread(lds + boff[c][0] + bo), trread(lds + boff[c][1] + bo));
    if (stats) F.sf = *(LAS bf16x8*)(xa + wn * 2048);
  };
  auto mm2 = [&](Frag& F) {
#pragma unroll
    for (int c = 0; c < 2; ++c) {
#pragma unroll
      for (int rt = 0; rt < 4; ++rt) acc[rt][c] = mfma16(F.a[rt], F.b[c], acc[rt][c]);
      cs[c] = mfma16(ones, F.b[c], cs[c]);
    }
    sa = mfma16(F.sf, ones, sa); sq = mfma16(F.sf, F.sf, sq);
  };
  auto mm1 = [&](Frag& F) {
#pragma unroll
    for (int c = 0; c < 2; ++c) {
#pragma unroll
      for (int rt = 0; rt < 4; ++rt) acc[rt][c] = mfma16(F.b[c], F.a[rt], acc[rt][c]);
      cs[c] = mfma16(F.b[c], ones, cs[c]);
    }
  };
  auto swap_keep = [&]() {
    if (wk) {
#pragma unroll
      for (int r = 0; r < 2; ++r)
#pragma unroll
        for (int c = 0; c < 2; ++c) { const f32x4 tmp = acc[r][c]; acc[r][c] = acc[2 + r][c]; acc[2 + r][c] = tmp; asm volatile("" : "+v"(acc[r][c]), "+v"(acc[2 + r][c])); }
    }
#pragma unroll
    for (int r = 0; r < 2; ++r)
#pragma unroll
      for (int c = 0; c < 2; ++c) scr[((wn * 2 + wk) * 4 + 2 * r + c) * 64 + lane] = acc[2 + r][c];
  };
#pragma unroll
  for (int rt = 0; rt < 4; ++rt)
#pragma unroll
    for (int c = 0; c < 2; ++c) acc[rt][c] = (f32x4){0, 0, 0, 0};
  cs[0] = (f32x4){0, 0, 0, 0}; cs[1] = (f32x4){0, 0, 0, 0};
  BAR();
  STAMP(1);
  rd(0, F0, true);
#pragma unroll 1
  for (int s = 0; s < 6; s += 2) {
    BAR(); rd(s + 1, F1, true); __builtin_amdgcn_sched_barrier(0); mm2(F0);
    BAR(); rd(s + 2, F0, true); __builtin_amdgcn_sched_barrier(0); mm2(F1);
  }
  BAR(); rd(7, F1, true); __builtin_amdgcn_sched_barrier(0); mm2(F0);
  BAR(); rd(8, F0, false); __builtin_amdgcn_sched_barrier(0); mm2(F1);
  STAMP(2);
  {
    LAS float* ST = (LAS float*)(lds + K1_ST);
    if (fr == 0) {
#pragma unroll
      for (int r = 0; r < 4; ++r) ST[(16 * wn + 4 * g + r) * 4 + 2 * wk] = sa[r];
    }
    const float dq = (fr & 2) ? ((fr & 1) ? sq[3] : sq[2]) : ((fr & 1) ? sq[1] : sq[0]);
    if ((fr >> 2) == g) ST[(16 * wn + fr) * 4 + 2 * wk + 1] = dq;
  }
  swap_keep();
  if (g == 0) { scs[((wn * 2 + wk) * 2 + 0) * 16 + fr] = cs[0][0]; scs[((wn * 2 + wk) * 2 + 1) * 16 + fr] = cs[1][0]; }
#pragma unroll
  for (int r = 0; r < 2; ++r)
#pragma unroll
    for (int c = 0; c < 2; ++c) za[r][c] = acc[r][c];
  zcs[0] = cs[0][0]; zcs[1] = cs[1][0];
#pragma unroll
  for (int rt = 0; rt < 4; ++rt)
#pragma unroll
    for (int c = 0; c < 2; ++c) acc[rt][c] = (f32x4){0, 0, 0, 0};
  cs[0] = (f32x4){0, 0, 0, 0}; cs[1] = (f32x4){0, 0, 0, 0};
  STAMP(3);
#pragma unroll
  for (int s = 8; s < 16; ++s) {
    Frag& Fc = (s & 1) ? F1 : F0;
    Frag& Fn = (s & 1) ? F0 : F1;
    f32x4 st = {0, 0, 0, 0};
    if (s < 15) BAR();
    if (s >= 9 && s < 13) st = *(LAS f32x4*)(lds + K1_S1 + (32 * wk + 16 * ((s - 9) >> 1) + 4 * g + 2 * ((s - 9) & 1)) * 8);
    if (s < 15) rd(s + 1, Fn, false);
    __builtin_amdgcn_sched_barrier(0);
    mm1(Fc);
    __builtin_amdgcn_sched_barrier(0);
    if (s == 8) {
#pragma unroll
      for (int r = 0; r < 2; ++r)
#pragma unroll
        for (int c = 0; c < 2; ++c) za[r][c] += scr[((wn * 2 + (wk ^ 1)) * 4 + 2 * r + c) * 64 + lane];
#pragma unroll
      for (int c = 0; c < 2; ++c) zcs[c] += scs[((wn * 2 + (wk ^ 1)) * 2 + c) * 16 + fr];
      if (w == 0) {
        const f32x4 v = *(LAS f32x4*)(lds + K1_ST + lane * 16);
        const float mean = (v[0] + v[2]) * (1.0f / 512.0f);
        const float var = __builtin_fmaxf((v[1] + v[3]) * (1.0f / 512.0f) - mean * mean, 0.f);
        const float den = rb(__builtin_sqrtf(rb(rb(var) + 1e-5f)));
        *(LAS f32x2*)(lds + K1_S1 + lane * 8) = (f32x2){rb(mean), __builtin_amdgcn_rcpf(den)};
      }
    }
    if (s >= 9 && s < 13) {
      const int rp = (s - 9) >> 1, r0 = 2 * ((s - 9) & 1);
      const int row = 32 * wk + 16 * rp + 4 * g + r0;
      float sm[2], sq2[2];
#pragma unroll
      for (int q = 0; q < 2; ++q) {
        f32x2 v = {za[rp][0][r0 + q], za[rp][1][r0 + q]};
        v = (v - st[2 * q] * (f32x2){zcs[0], zcs[1]}) * st[2 * q + 1];
        v = rb2(gelu2(v));
        za[rp][0][r0 + q] = v[0]; za[rp][1][r0 + q] = v[1];
        sm[q] = v[0] + v[1]; sq2[q] = v[0] * v[0] + v[1] * v[1];
      }
      sm[0] = dpp_add<0xB1>(sm[0]); sm[1] = dpp_add<0xB1>(sm[1]); sq2[0] = dpp_add<0xB1>(sq2[0]); sq2[1] = dpp_add<0xB1>(sq2[1]);
      sm[0] = dpp_add<0x4E>(sm[0]); sm[1] = dpp_add<0x4E>(sm[1]); sq2[0] = dpp_add<0x4E>(sq2[0]); sq2[1] = dpp_add<0x4E>(sq2[1]);
      sm[0] = dpp_add<0x141>(sm[0]); sm[1] = dpp_add<0x141>(sm[1]); sq2[0] = dpp_add<0x141>(sq2[0]); sq2[1] = dpp_add<0x141>(sq2[1]);
      sm[0] = dpp_add<0x140>(sm[0]); sm[1] = dpp_add<0x140>(sm[1]); sq2[0] = dpp_add<0x140>(sq2[0]); sq2[1] = dpp_add<0x140>(sq2[1]);
      if (fr == 0) {
        *(LAS f32x2*)(lds + K1_P + (row * 4 + wn) * 8) = (f32x2){sm[0], sq2[0]};
        *(LAS f32x2*)(lds + K1_P + ((row + 1) * 4 + wn) * 8) = (f32x2){sm[1], sq2[1]};
      }
    }
    if (s == 13 && w == 0) {
      const f32x4 pa = *(LAS f32x4*)(lds + K1_P + lane * 32), pc = *(LAS f32x4*)(lds + K1_P + lane * 32 + 16);
      const float m0 = pa[0] * (1.0f / 32.0f), m1 = pa[2] * (1.0f / 32.0f), m2_ = pc[0] * (1.0f / 32.0f), m3 = pc[2] * (1.0f / 32.0f);
      const float mj = (m0 + m1 + m2_ + m3) * 0.25f;
      const float w2 = __builtin_fmaxf(pa[1] - m0 * pa[0], 0.f) + __builtin_fmaxf(pa[3] - m1 * pa[2], 0.f) + __builtin_fmaxf(pc[1] - m2_ * pc[0], 0.f) + __builtin_fmaxf(pc[3] - m3 * pc[2], 0.f);
      const float da = m0 - mj, db = m1 - mj, dc = m2_ - mj, dd = m3 - mj;
      const float m2 = w2 + 32.0f * ((da * da + db * db) + (dc * dc + dd * dd));
      gu64* slot = (gu64*)(p.ws + WS_XCH) + ((size_t)(ib * 7 + jb) * 64 + lane) * 2;
      __hip_atomic_store(slot, ((unsigned long long)tag << 32) | __builtin_bit_cast(unsigned, mj), RLX_AGENT);
      __hip_atomic_store(slot + 1, ((unsigned long long)tag << 32) | __builtin_bit_cast(unsigned, m2), RLX_AGENT);
    }
    if (s == 14) {
      const int nblk = jb < 4 ? 5 : 4;
      for (int u = w; u < 2 * nblk; u += 8) {
        const int dblk = jb + 7 * (u >> 1), t0 = 32 * (u & 1) + 8 * g;
        const int chunk = 2 * (dblk & 3) + ((fr & 3) >> 1);
        s16x4 h[2];
#pragma unroll
        for (int tt = 0; tt < 2; ++tt) {
          const int row = t0 + 4 * tt + (fr >> 2);
          h[tt] = trread(lds + (dblk >> 2) * 8192 + row * 128 + ((chunk ^ ((row >> 1) & 7)) * 16) + 8 * (fr & 1));
        }
        const u32x2 h0 = __builtin_bit_cast(u32x2, h[0]), h1 = __builtin_bit_cast(u32x2, h[1]);
        u32x4 o;
#pragma unroll
        for (int e = 0; e < 4; ++e) {
          const f32x4 st = *(LAS f32x4*)(lds + K1_S1 + (t0 + 2 * e) * 8);
          const unsigned xw = e < 2 ? h0[e] : h1[e - 2];
          const f32x2 dv = rb2((f32x2){bflo(xw) - st[0], bfhi(xw) - st[2]});
          o[e] = pk2(dv[0] * st[1], dv[1] * st[3]);
        }
        *(u32x4*)(p.ws + WS_XNT + ((size_t)(16 * dblk + fr) * 2048 + 64 * ib + t0) * 2) = o;
      }
    }
  }
  STAMP(4);
  BAR();
  swap_keep();
  if (fr == 0) {
#pragma unroll
    for (int c = 0; c < 2; ++c)
#pragma unroll
      for (int r = 0; r < 4; ++r) scs[((wn * 2 + wk) * 2 + c) * 16 + 4 * g + r] = cs[c][r];
  }
  BAR();
  STAMP(5);
#pragma unroll
  for (int rp = 0; rp < 2; ++rp) {
    const f32x2 st = *(LAS f32x2*)(lds + K1_S1 + (32 * wk + 16 * rp + fr) * 8);
    u32x4 o;
#pragma unroll
    for (int c = 0; c < 2; ++c) {
      const f32x4 v = acc[rp][c] + scr[((wn * 2 + (wk ^ 1)) * 4 + 2 * rp + c) * 64 + lane];
      const f32x4 cc = cs[c] + *(LAS f32x4*)(scs + ((wn * 2 + (wk ^ 1)) * 2 + c) * 16 + 4 * g);
#pragma unroll
      for (int h = 0; h < 2; ++h) {
        f32x2 z = {v[2 * h], v[2 * h + 1]};
        z = (z - st[0] * (f32x2){cc[2 * h], cc[2 * h + 1]}) * st[1];
        z = gelu2(z);
        o[2 * c + h] = pk2(z[0], z[1]);
      }
    }
    const int t16 = 4 * ib + 2 * wk + rp, cp = 4 * jb + wn;
    *(u32x4*)(p.ws + WS_Z1F + ((size_t)(t16 * 28 + cp) * 64 + lane) * 16) = o;
  }
  STAMP(6);
  if (w == 0) {
    gu64* slot = (gu64*)(p.ws + WS_XCH) + ((size_t)(ib * 7) * 64 + lane) * 2;
    float mj[7], m2j[7];
    for (unsigned spins = 0; spins < (1u << 22); ++spins) {
      bool ok = true;
#pragma unroll
      for (int j = 0; j < 7; ++j) {
        const unsigned long long u0 = __hip_atomic_load(slot + j * 128, RLX_AGENT), u1 = __hip_atomic_load(slot + j * 128 + 1, RLX_AGENT);
        mj[j] = __builtin_bit_cast(float, (unsigned)u0); m2j[j] = __builtin_bit_cast(float, (unsigned)u1);
        ok &= ((unsigned)(u0 >> 32) == tag) & ((unsigned)(u1 >> 32) == tag);
      }
      if (__all(ok)) break;
      __builtin_amdgcn_s_sleep(1);
    }
    float mean = 0.f;
#pragma unroll
    for (int j = 0; j < 7; ++j) mean += mj[j];
    mean *= (1.0f / 7.0f);
    float m2 = 0.f;
#pragma unroll
    for (int j = 0; j < 7; ++j) { const float dm = mj[j] - mean; m2 += m2j[j] + 128.0f * dm * dm; }
    const float var = m2 * (1.0f / 896.0f);
    const float den = rb(__builtin_sqrtf(rb(rb(var) + 1e-5f)));
    *(LAS f32x2*)(lds + K1_S2 + lane * 8) = (f32x2){rb(mean), __builtin_amdgcn_rcpf(den)};
  }
  BAR();
  STAMP(7);
  {
#pragma unroll
    for (int c = 0; c < 2; ++c) {
      u32x4 o;
#pragma unroll
      for (int rp = 0; rp < 2; ++rp) {
        const f32x4 s01 = *(LAS f32x4*)(lds + K1_S2 + (32 * wk + 16 * rp + 4 * g) * 8), s23 = *(LAS f32x4*)(lds + K1_S2 + (32 * wk + 16 * rp + 4 * g + 2) * 8);
        const f32x2 d0 = {za[rp][c][0] - s01[0], za[rp][c][1] - s01[2]}, d1 = {za[rp][c][2] - s23[0], za[rp][c][3] - s23[2]};
        o[2 * rp] = pk2(d0[0] * s01[1], d0[1] * s01[3]); o[2 * rp + 1] = pk2(d1[0] * s23[1], d1[1] * s23[3]);
      }
      const int ks = 2 * ib + wk, c16 = 8 * jb + 2 * wn + c;
      *(u32x4*)(p.ws + WS_Z2F + ((size_t)(ks * 56 + c16) * 64 + lane) * 16) = o;
    }
  }
  if (b == 0 && w == 1 && lane < 40) *(unsigned*)(p.ws + WS_CNT + lane * 128) = 0u;
  if (b == 0 && w == 0) {
    gu64* slot = (gu64*)(p.ws + WS_XCH);
    for (unsigned spins = 0; spins < (1u << 22); ++spins) {
      bool ok = true;
#pragma unroll
      for (int q = 0; q < 4; ++q) {
        const int ij = lane + 64 * q;
        if (ij < 224) ok &= ((unsigned)(__hip_atomic_load(slot + (size_t)ij * 128, RLX_AGENT) >> 32) == tag);
      }
      if (__all(ok)) break;
      __builtin_amdgcn_s_sleep(1);
    }
    if (lane == 0) __hip_atomic_store((gu32*)(p.ws + WS_EP), tag, RLX_AGENT);
  }
  STAMP(8);
  STAMP_FLUSH(0, 9);
}

constexpr int K2_A = 0, K2_U = 65536, K2_TAB = K2_U + 2 * 4608  , K2_TABW = 64 * 272  , K2_RCS = 4160, K2_RCCH = 4 * K2_RCS  ,
              K2_PART = K2_TAB + 2 * K2_RCCH  , K2_G = K2_TAB + 4 * K2_TABW  , K2_RED2 = K2_PART + 32768  ,
              K2_FLG = 163840 - 64, K2_LDS = 163840;
constexpr int K2_RED3 = 0;

__device__ __forceinline__ f32x2 cmul(f32x2 a, f32x2 b) {
  const f32x2 t = (f32x2){a[0], a[0]} * b;
  return (f32x2){a[1], a[1]} * (f32x2){-b[1], b[0]} + t;
}
__device__ __forceinline__ void wait_flags(LAS char* lds, int step, int mask) {
  LAS int* flg = (LAS int*)(lds + K2_FLG);
  for (;;) {
    const u32x4 v0 = *(volatile LAS u32x4*)flg, v1 = *(volatile LAS u32x4*)(flg + 4);
    bool ok = true;
#pragma unroll
    for (int i = 0; i < 4; ++i) ok &= ((!((mask >> i) & 1)) || ((int)v0[i] >= step)) & ((!((mask >> (4 + i)) & 1)) || ((int)v1[i] >= step));
    if (__builtin_amdgcn_readfirstlane(ok)) break;
    __builtin_amdgcn_s_sleep(1);
  }
  asm volatile("" ::: "memory");
}
__device__ __forceinline__ void group_sync(LAS char* lds, int w, int step, int mask, int lane) {
  asm volatile("s_waitcnt lgkmcnt(0)" ::: "memory");
  if (lane == 0) __hip_atomic_store((LAS int*)(lds + K2_FLG) + w, step, __ATOMIC_RELAXED, __HIP_MEMORY_SCOPE_WORKGROUP);
  wait_flags(lds, step, mask);
}
__device__ __forceinline__ LAS char* redslot(LAS char* lds, int slot) { return lds + (slot < 9 ? slot * 7168 : K2_RED2 + (slot - 9) * 7168); }

extern "C" __global__ __launch_bounds__(512, 2) void k2_kernel(Params p) {
  extern __shared__ __attribute__((aligned(16))) char smem[];
  LAS char* lds = (LAS char*)smem;
  const int tid = threadIdx.x, lane = tid & 63, w = __builtin_amdgcn_readfirstlane(tid >> 6);
  const int b = blockIdx.x, xcd = b & 7, idx = b >> 3;
  const int ib = (xcd >> 1) * 8 + (idx & 7), jb = (xcd & 1) * 4 + (idx >> 3);
  const int fr = lane & 15, g = lane >> 4;
  STAMP_DECL
  STAMP(0);
  if (tid < 8) *(LAS int*)(lds + K2_FLG + tid * 4) = 0;
  u16 prm[6] = {0, 0, 0, 0, 0, 0};
  u32x4 u0 = {0, 0, 0, 0}, u1 = {0, 0, 0, 0};
  if (w >= 4) {
    const int sw = w - 4, d = 2 * b + (sw >> 1), n = 64 * (sw & 1) + lane;
    prm[0] = p.a_re[d * 128 + n]; prm[1] = p.a_im[d * 128 + n]; prm[2] = p.b_re[d * 128 + n]; prm[3] = p.b_im[d * 128 + n];
    prm[4] = p.c_re[d * 128 + n]; prm[5] = p.c_im[d * 128 + n];
    u0 = *(const u32x4*)(p.ws + WS_XNT + ((size_t)d * 2048 + 1024 * (sw & 1)) * 2 + lane * 16);
    u1 = *(const u32x4*)(p.ws + WS_XNT + ((size_t)d * 2048 + 1024 * (sw & 1) + 512) * 2 + lane * 16);
  }
  BAR();
  const __amdgpu_buffer_rsrc_t hrs = __builtin_amdgcn_make_buffer_rsrc(p.ws + WS_HF, 0, 32 * 8 * 4 * 3584, 0x00020000);
  const __amdgpu_buffer_rsrc_t yrs = __builtin_amdgcn_make_buffer_rsrc(p.ws + WS_YA2, 0, 256 * 8192, 0x00020000);
  gu32* cntH = (gu32*)(p.ws + WS_CNT) + ib * 32;
  gu32* cntY = (gu32*)(p.ws + WS_CNT) + (32 + jb) * 32;
  if (w < 4) {
    const int cw = w;
    f32x4 acc[4][7];
#pragma unroll
    for (int r = 0; r < 4; ++r)
#pragma unroll
      for (int c = 0; c < 7; ++c) acc[r][c] = (f32x4){0, 0, 0, 0};
    const int r8 = lane >> 3;
    unsigned voffA[2];
#pragma unroll
    for (int par = 0; par < 2; ++par) voffA[par] = (unsigned)r8 * 4096u + (unsigned)(((lane & 7) ^ (((r8 >> 1) + 4 * par) & 7)) * 16);
    const char* gA = (const char*)p.w_sp + (size_t)(64 * ib) * 4096;
    const char* gB = p.ws + WS_Z2F + (size_t)(7 * jb + 3) * 1024;
    const unsigned lane16 = lane * 16;
    LAS char* abuf = lds + K2_A + cw * 16384;
    auto issueA = [&](int gl) {
      const char* base = gA + (4 * gl + cw) * 128;
#pragma unroll
      for (int n = 0; n < 8; ++n) GLDS16(base + n * 32768 + voffA[n & 1], abuf + (gl & 1) * 8192 + n * 1024);
    };
    u32x4 bq[3][7];
    auto issueB = [&](int s, u32x4 (&dst)[7]) {
      const char* base = gB + (size_t)(2 * (4 * (s >> 1) + cw) + (s & 1)) * (56 * 1024);
#pragma unroll
      for (int c = 0; c < 7; ++c) GLOAD16(dst[c], lane16, base, (c - 3) * 1024);
    };
    unsigned afr[2][2];
#pragma unroll
    for (int kk = 0; kk < 2; ++kk)
#pragma unroll
      for (int pp = 0; pp < 2; ++pp) afr[kk][pp] = ldsaddr(abuf) + fr * 128 + (((4 * kk + 2 * pp + (g >> 1)) ^ ((fr >> 1) & 7)) * 16) + (g & 1) * 8;
    issueA(0); issueB(0, bq[0]); issueB(1, bq[1]); issueA(1); issueB(2, bq[2]);
#pragma unroll
    for (int s = 0; s < 16; ++s) {
      u32x4 (&bc)[7] = bq[s % 3];
      if (s <= 13) asm volatile("s_waitcnt vmcnt(22)" : "+v"(bc[0]), "+v"(bc[1]), "+v"(bc[2]), "+v"(bc[3]), "+v"(bc[4]), "+v"(bc[5]), "+v"(bc[6])::"memory");
      else if (s == 14) asm volatile("s_waitcnt vmcnt(7)" : "+v"(bc[0]), "+v"(bc[1]), "+v"(bc[2]), "+v"(bc[3]), "+v"(bc[4]), "+v"(bc[5]), "+v"(bc[6])::"memory");
      else asm volatile("s_waitcnt vmcnt(0)" : "+v"(bc[0]), "+v"(bc[1]), "+v"(bc[2]), "+v"(bc[3]), "+v"(bc[4]), "+v"(bc[5]), "+v"(bc[6])::"memory");
      s16x4 lo[4], hi[4];
#pragma unroll
      for (int rt = 0; rt < 4; ++rt) {
        asm volatile("ds_read_b64 %0, %1 offset:%2" : "=v"(lo[rt]) : "v"(afr[s & 1][0]), "i"(((s >> 1) & 1) * 8192 + rt * 2048) : "memory");
        asm volatile("ds_read_b64 %0, %1 offset:%2" : "=v"(hi[rt]) : "v"(afr[s & 1][1]), "i"(((s >> 1) & 1) * 8192 + rt * 2048) : "memory");
      }
#pragma unroll
      for (int rt = 0; rt < 4; ++rt) {
        if (rt == 0) asm volatile("s_waitcnt lgkmcnt(6)" : "+v"(lo[0]), "+v"(hi[0])::"memory");
        else if (rt == 1) asm volatile("s_waitcnt lgkmcnt(4)" : "+v"(lo[1]), "+v"(hi[1])::"memory");
        else if (rt == 2) asm volatile("s_waitcnt lgkmcnt(2)" : "+v"(lo[2]), "+v"(hi[2])::"memory");
        else asm volatile("s_waitcnt lgkmcnt(0)" : "+v"(lo[3]), "+v"(hi[3])::"memory");
        const bf16x8 a = cat8(lo[rt], hi[rt]);
#pragma unroll
        for (int c = 0; c < 7; ++c) acc[rt][c] = mfma16(__builtin_bit_cast(bf16x8, bc[c]), a, acc[rt][c]);
      }
      __builtin_amdgcn_sched_barrier(0);
      if (s + 3 < 16) {
        if (((s + 3) & 1) == 0) issueA((s + 3) >> 1);
        issueB(s + 3, bc);
      }
    }
    STAMP(1);
    const int t = 64 * ib + 16 * cw + fr, t16 = 4 * ib + cw;
    u32x2 z1v[7];
#pragma unroll
    for (int c = 0; c < 7; ++c) {
      const int c16 = 7 * jb + c;
      z1v[c] = *(const u32x2*)(p.ws + WS_Z1F + ((size_t)(t16 * 28 + (c16 >> 1)) * 64 + lane) * 16 + (c16 & 1) * 8);
    }
    const u16 bsv = p.b_sp[t];
    group_sync(lds, w, 1, 0x0f, lane);
    wait_flags(lds, 4, 0xf0);
#pragma unroll
    for (int rt = 0; rt < 4; ++rt)
      if (rt != cw) {
        LAS f32x4* red = (LAS f32x4*)redslot(lds, rt * 3 + (cw > rt ? cw - 1 : cw));
#pragma unroll
        for (int c = 0; c < 7; ++c) red[c * 64 + lane] = acc[rt][c];
      }
    f32x4 mine[7];
#pragma unroll
    for (int c = 0; c < 7; ++c) mine[c] = acc[0][c];
    if (cw == 1) {
#pragma unroll
      for (int c = 0; c < 7; ++c) { mine[c] = acc[1][c]; asm volatile("" : "+v"(mine[c])); }
    } else if (cw == 2) {
#pragma unroll
      for (int c = 0; c < 7; ++c) { mine[c] = acc[2][c]; asm volatile("" : "+v"(mine[c])); }
    } else if (cw == 3) {
#pragma unroll
      for (int c = 0; c < 7; ++c) { mine[c] = acc[3][c]; asm volatile("" : "+v"(mine[c])); }
    }
    group_sync(lds, w, 2, 0x0f, lane);
    STAMP(2);
    {
#pragma unroll
      for (int k2 = 0; k2 < 3; ++k2) {
        LAS f32x4* red = (LAS f32x4*)redslot(lds, cw * 3 + k2);
#pragma unroll
        for (int c = 0; c < 7; ++c) mine[c] += red[c * 64 + lane];
      }
      const float bs = bf2f(bsv);
      unsigned hw[7][2];
#pragma unroll
      for (int c = 0; c < 7; ++c) {
        const f32x4 v = mine[c];
        const f32x2 g01 = rb2(rb2((f32x2){v[0], v[1]}) + bs), g23 = rb2(rb2((f32x2){v[2], v[3]}) + bs);
        hw[c][0] = pk2(bflo(z1v[c][0]) * g01[0], bfhi(z1v[c][0]) * g01[1]); hw[c][1] = pk2(bflo(z1v[c][1]) * g23[0], bfhi(z1v[c][1]) * g23[1]);
      }
      const int hb = ((ib * 8 + jb) * 4 + cw) * 3584;
#pragma unroll
      for (int m = 0; m < 3; ++m)
        __builtin_amdgcn_raw_buffer_store_b128((u32x4){hw[2 * m][0], hw[2 * m][1], hw[2 * m + 1][0], hw[2 * m + 1][1]}, hrs, hb + m * 1024 + lane * 16, 0, 16);
      __builtin_amdgcn_raw_buffer_store_b64((u32x2){hw[6][0], hw[6][1]}, hrs, hb + 3072 + lane * 8, 0, 16);
      WAIT_V(0);
    }
    group_sync(lds, w, 3, 0x0f, lane);
    if (tid == 0) __hip_atomic_fetch_add(cntH, 1u, RLX_AGENT);
    {
      const int r8b = lane >> 3;
      const char* src = (const char*)p.w_out + (size_t)r8b * 1024 + jb * 128 + (((lane & 7) ^ (2 * ((r8b >> 1) & 3))) * 16);
#pragma unroll
      for (int n = 0; n < 16; ++n) { const int pc = cw * 16 + n; GLDS16(src + (size_t)pc * 8192, lds + pc * 1024); }
    }
    STAMP(3);
    WAIT_V(0);
  } else {
    const int sw = w - 4, ch = sw >> 1, hh = sw & 1;
    const int pm = 0x30 << (2 * ch);
    const float are = bf2f(prm[0]), aim = bf2f(prm[1]), bre = bf2f(prm[2]), bim = bf2f(prm[3]), cre = bf2f(prm[4]), cim = bf2f(prm[5]);
    const unsigned ld2 = *(const CAS unsigned*)(p.log_dt + 2 * b);
    const unsigned dk2 = *(const CAS unsigned*)(p.dskip + 2 * b);
    const float dt = rb(expf(ch ? bfhi(ld2) : bflo(ld2)));
    LAS char* tab = lds + K2_TAB + sw * K2_TABW;
    STAMP(1);
    {
      const float lr = dt * are * 1.4426950408889634f, fq = dt * aim * 0.15915494309189535f;
      const float dbr = dt * bre, dbi = dt * bim;
      const f32x2 W = {cre * dbr - cim * dbi, cre * dbi + cim * dbr};
      auto cx = [&](float a) {
        const float m = __builtin_amdgcn_exp2f(lr * a), ph = __builtin_amdgcn_fractf(fq * a);
        return (f32x2){m * __builtin_amdgcn_cosf(ph), m * __builtin_amdgcn_sinf(ph)};
      };
      auto put = [&](int row, f32x2 v) { *(LAS unsigned*)(tab + row * 272 + lane * 4) = pk2(v[0], v[1]); };
      const f32x2 E1 = cx(1.f), E8 = cx(8.f), E128 = cx(128.f);
      f32x2 P[8];
      P[0] = (f32x2){1.f, 0.f}; P[1] = E1;
#pragma unroll
      for (int r = 2; r < 8; ++r) P[r] = cmul(P[r - 1], E1);
#pragma unroll
      for (int r = 0; r < 8; ++r) { put(r, P[r]); put(8 + r, cmul(P[r], E128)); }
      f32x2 F = cmul(E128, E128);
#pragma unroll
      for (int m = 0; m < 2; ++m) {
#pragma unroll
        for (int r = 0; r < 4; ++r) put(16 + 4 * m + r, r == 0 ? F : cmul(F, P[2 * r]));
        F = cmul(F, E128);
      }
#pragma unroll
      for (int m = 0; m < 4; ++m) {
        put(24 + 2 * m, F); put(25 + 2 * m, cmul(F, P[4]));
        F = cmul(F, E128);
      }
#pragma unroll
      for (int m = 0; m < 9; ++m) {
        put(32 + m, F);
        if (m < 8) F = cmul(F, E128);
      }
      f32x2 Y = W;
#pragma unroll
      for (int c = 0; c < 16; ++c) {
        put(48 + c, (f32x2){Y[0], -Y[1]});
        if (c < 15) Y = cmul(Y, E8);
      }
    }
    *(LAS u32x4*)(lds + K2_U + ch * 4608 + 512 + hh * 2048 + lane * 16) = u0;
    *(LAS u32x4*)(lds + K2_U + ch * 4608 + 512 + hh * 2048 + 1024 + lane * 16) = u1;
    if (hh == 0 && lane < 32) *(LAS u32x4*)(lds + K2_U + ch * 4608 + lane * 16) = (u32x4){0, 0, 0, 0};
    STAMP(2);
    f32x4 ka[3] = {{0, 0, 0, 0}, {0, 0, 0, 0}, {0, 0, 0, 0}};
#pragma unroll
    for (int ks = 0; ks < 4; ++ks) {
      const bf16x8 yf = *(LAS bf16x8*)(tab + (48 + fr) * 272 + ks * 64 + g * 16);
#pragma unroll
      for (int t = 0; t < 3; ++t) ka[t] = mfma16(*(LAS bf16x8*)(tab + (16 * t + fr) * 272 + ks * 64 + g * 16), yf, ka[t]);
    }
    if (hh == 0) *(LAS f32x4*)(tab + lane * 16) = ka[1];
    else { *(LAS f32x4*)(tab + lane * 16) = ka[0]; *(LAS f32x4*)(tab + 1024 + lane * 16) = ka[2]; }
    group_sync(lds, w, 1, pm, lane);
    STAMP(3);
    LAS char* T = lds + K2_TAB + (2 * ch) * K2_TABW + 4096;
    {
      LAS char* oth = lds + K2_TAB + (sw ^ 1) * K2_TABW;
      if (hh == 0) {
        const f32x4 s0 = ka[0] + *(LAS f32x4*)(oth + lane * 16), s2 = ka[2] + *(LAS f32x4*)(oth + 1024 + lane * 16);
        LAS u16* t0 = (LAS u16*)T + 4 * (g & 1) + 128 * (g >> 1) + 8 * fr;
#pragma unroll
        for (int r = 0; r < 4; ++r) t0[r] = (u16)(pk2(s0[r], 0.f) & 0xffffu);
        LAS u16* t2 = (LAS u16*)T + 1024 + 512 * g + 8 * fr;
        if (g < 2) {
#pragma unroll
          for (int r = 0; r < 4; ++r) t2[128 * r] = (u16)(pk2(s2[r], 0.f) & 0xffffu);
        } else if (g == 2) t2[0] = (u16)(pk2(s2[0], 0.f) & 0xffffu);
      } else {
        const f32x4 s1 = ka[1] + *(LAS f32x4*)(oth + lane * 16);
        LAS u16* t1 = (LAS u16*)T + (g < 2 ? 256 + 128 * g : 512 + 256 * (g - 2)) + 8 * fr;
        const int o1 = g < 2 ? 2 : 4, o2 = g < 2 ? 4 : 128, o3 = g < 2 ? 6 : 132;
        t1[0] = (u16)(pk2(s1[0], 0.f) & 0xffffu); t1[o1] = (u16)(pk2(s1[1], 0.f) & 0xffffu);
        t1[o2] = (u16)(pk2(s1[2], 0.f) & 0xffffu); t1[o3] = (u16)(pk2(s1[3], 0.f) & 0xffffu);
      }
    }
    group_sync(lds, w, 2, pm, lane);
    STAMP(4);
    LAS char* G = lds + K2_G + ch * 4128;
#pragma unroll
    for (int it = 0; it < 2; ++it) {
      const int gc = 128 * hh + 64 * it + lane;
      u32x4 o;
#pragma unroll
      for (int e = 0; e < 8; e += 2) {
        const int l0 = 2047 - (8 * gc + e), l1 = l0 - 1;
        const unsigned cv = pk2((float)l0, (float)l1);
        const int i0 = (int)bflo(cv), i1 = (int)bfhi(cv);
        o[e >> 1] = (unsigned)((LAS u16*)T)[i0] | ((unsigned)((LAS u16*)T)[i1] << 16);
      }
      *(LAS u32x4*)(G + gc * 16) = o;
    }
    if (hh == 0 && lane == 0) *(LAS u32x4*)(G + 4096) = (u32x4){0, 0, 0, 0};
    group_sync(lds, w, 3, 0xf0, lane);
    STAMP(5);
    LAS char* rc = lds + K2_TAB + ch * K2_RCCH;
#pragma unroll
    for (int it = 0; it < 2; ++it) {
      const int gc = 128 * hh + 64 * it + lane;
      const u32x4 g0 = *(LAS u32x4*)(G + gc * 16), g1 = *(LAS u32x4*)(G + gc * 16 + 16);
      const unsigned wd[8] = {g0[0], g0[1], g0[2], g0[3], g1[0], g1[1], g1[2], g1[3]};
#pragma unroll
      for (int c = 0; c < 4; ++c) {
        const int s = 3 - c;
        u32x4 o;
#pragma unroll
        for (int i = 0; i < 4; ++i) o[i] = (s & 1) ? __builtin_amdgcn_alignbit(wd[(s >> 1) + i + 1], wd[(s >> 1) + i], 16) : wd[(s >> 1) + i];
        *(LAS u32x4*)(rc + c * K2_RCS + gc * 16) = o;
      }
    }
    if (hh == 0 && lane < 8) *(LAS u32x4*)(rc + (lane & 3) * K2_RCS + 4096 + (lane >> 2) * 16) = (u32x4){0, 0, 0, 0};
    group_sync(lds, w, 4, pm, lane);
    STAMP(6);
    f32x4 cacc[8];
#pragma unroll
    for (int r = 0; r < 8; ++r) cacc[r] = (f32x4){0, 0, 0, 0};
    LAS char* ub = lds + K2_U + ch * 4608 + (240 + 16 * fr + 8 * g) * 2;
    LAS char* kb = rc + (fr & 3) * K2_RCS + (2028 + 8 * g - 4 * (fr >> 2)) * 2;
#pragma unroll 1
    for (int pp = 0; pp < 4; ++pp) {
      const int p0 = 4 * hh + pp;
      bf16x8 ua[8], kf[8];
#pragma unroll
      for (int r = 0; r < 8; ++r) {
        LAS char* ka_ = kb - (p0 + 8 * r) * 64;
        kf[r] = cat8(*(LAS s16x4*)ka_, *(LAS s16x4*)(ka_ + 8));
        ua[r] = *(LAS bf16x8*)(ub + (8 * r - p0) * 64);
      }
      __builtin_amdgcn_sched_barrier(0);
#pragma unroll
      for (int i = 0; i < 8; ++i)
#pragma unroll
        for (int rt = i; rt < 8; ++rt) cacc[rt] = mfma16(ua[rt - i], kf[i], cacc[rt]);
    }
    STAMP(7);
    {
      LAS float* part = (LAS float*)(lds + K2_PART + sw * 8192);
#pragma unroll
      for (int rt = 0; rt < 8; ++rt)
#pragma unroll
        for (int r = 0; r < 4; ++r) part[256 * rt + 64 * g + 16 * r + fr] = cacc[rt][r];
    }
    group_sync(lds, w, 5, 0xf0, lane);
    STAMP(8);
    {
      const int tt = sw * 64 + lane;
      unsigned yb[2][4];
#pragma unroll
      for (int c2 = 0; c2 < 2; ++c2) {
        LAS f32x4* q0 = (LAS f32x4*)(lds + K2_PART + (2 * c2) * 8192 + tt * 32);
        LAS f32x4* q1 = (LAS f32x4*)(lds + K2_PART + (2 * c2 + 1) * 8192 + tt * 32);
        const f32x4 s0 = q0[0] + q1[0], s1 = q0[1] + q1[1];
        const u32x4 uv = *(LAS u32x4*)(lds + K2_U + c2 * 4608 + 512 + tt * 16);
        const float dsk = c2 ? bfhi(dk2) : bflo(dk2);
#pragma unroll
        for (int e = 0; e < 4; ++e) {
          const f32x2 y = rb2(e < 2 ? (f32x2){s0[2 * e], s0[2 * e + 1]} : (f32x2){s1[2 * e - 4], s1[2 * e - 3]});
          const f32x2 ud = rb2((f32x2){bflo(uv[e]) * dsk, bfhi(uv[e]) * dsk});
          yb[c2][e] = pk2(y[0] + ud[0], y[1] + ud[1]);
        }
      }
      u32x4 o0, o1;
#pragma unroll
      for (int e = 0; e < 2; ++e) {
        o0[2 * e] = (yb[0][e] & 0xffffu) | (yb[1][e] << 16); o0[2 * e + 1] = (yb[0][e] >> 16) | (yb[1][e] & 0xffff0000u);
        o1[2 * e] = (yb[0][2 + e] & 0xffffu) | (yb[1][2 + e] << 16); o1[2 * e + 1] = (yb[0][2 + e] >> 16) | (yb[1][2 + e] & 0xffff0000u);
      }
      __builtin_amdgcn_raw_buffer_store_b128(o0, yrs, b * 8192 + tt * 32, 0, 16);
      __builtin_amdgcn_raw_buffer_store_b128(o1, yrs, b * 8192 + tt * 32 + 16, 0, 16);
      WAIT_V(0);
    }
    group_sync(lds, w, 6, 0xf0, lane);
    if (sw == 0 && lane == 0) __hip_atomic_fetch_add((gu32*)(p.ws + WS_CNT) + (32 + (b >> 5)) * 32, 1u, RLX_AGENT);
    STAMP(9);
    {
      const int r8 = lane >> 3;
      const char* src = (const char*)p.w_out + (size_t)r8 * 1024 + jb * 128 + (((lane & 7) ^ (2 * ((r8 >> 1) & 3))) * 16);
#pragma unroll
      for (int n = 0; n < 12; ++n) { const int pc = 64 + sw * 12 + n; GLDS16(src + (size_t)pc * 8192, lds + pc * 1024); }
    }
    STAMP(10);
    if (sw == 0) {
      for (unsigned spins = 0; spins < (1u << 22); ++spins) {
        const unsigned a = __hip_atomic_load(cntH, RLX_AGENT), c = __hip_atomic_load(cntY, RLX_AGENT);
        if (__builtin_amdgcn_readfirstlane((a >= 8u) & (c >= 32u))) break;
        __builtin_amdgcn_s_sleep(1);
      }
    }
    WAIT_V(0);
  }
  BAR();
  STAMP(11);
  {
    const int wm = w & 1, wk = w >> 1;
    u32x4 hf[2][2][3]; u32x2 hh2[2][2];
#pragma unroll
    for (int r = 0; r < 2; ++r)
#pragma unroll
      for (int s = 0; s < 2; ++s) {
        const int hb = ((ib * 8 + 2 * wk + s) * 4 + 2 * wm + r) * 3584;
#pragma unroll
        for (int m = 0; m < 3; ++m) hf[r][s][m] = __builtin_amdgcn_raw_buffer_load_b128(hrs, hb + m * 1024 + lane * 16, 0, 16);
        hh2[r][s] = __builtin_amdgcn_raw_buffer_load_b64(hrs, hb + 3072 + lane * 8, 0, 16);
      }
    const int er = wk >> 1, cg = wk & 1, ert = 2 * wm + er;
    const int t = 64 * ib + 16 * ert + fr, d0 = 64 * jb + 32 * cg + 8 * g;
    const u32x4 xv = *(const u32x4*)(p.x + (size_t)t * 512 + d0);
    unsigned ya[4];
#pragma unroll
    for (int e = 0; e < 4; ++e) ya[e] = __builtin_amdgcn_raw_buffer_load_b32(yrs, ((d0 / 2 + e) * 2048 + t) * 4, 0, 16);
    f32x4 acc[2][4];
#pragma unroll
    for (int r = 0; r < 2; ++r)
#pragma unroll
      for (int c = 0; c < 4; ++c) acc[r][c] = (f32x4){0, 0, 0, 0};
    auto wadr = [&](int frow, int c) {
      const int row = frow + 4 * g + (fr >> 2);
      const int col = 32 * (c >> 1) + 8 * (fr & 3) + 4 * (c & 1);
      const int chunk = col >> 3;
      return lds + row * 128 + ((chunk ^ (2 * ((row >> 1) & 3))) * 16) + (col & 4) * 2;
    };
#pragma unroll
    for (int s = 0; s < 2; ++s) {
      const int f0 = 112 * (2 * wk + s);
#pragma unroll
      for (int m = 0; m < 3; ++m)
#pragma unroll
        for (int c = 0; c < 4; ++c) {
          const bf16x8 wf = cat8(trread(wadr(f0 + 32 * m, c)), trread(wadr(f0 + 32 * m + 16, c)));
#pragma unroll
          for (int r = 0; r < 2; ++r) acc[r][c] = mfma16(wf, __builtin_bit_cast(bf16x8, hf[r][s][m]), acc[r][c]);
        }
#pragma unroll
      for (int c = 0; c < 4; ++c) {
        const s16x4 wh = trread(wadr(f0 + 96, c));
#pragma unroll
        for (int r = 0; r < 2; ++r) acc[r][c] = __builtin_amdgcn_mfma_f32_16x16x16bf16_1k(wh, __builtin_bit_cast(s16x4, hh2[r][s]), acc[r][c], 0, 0, 0);
      }
    }
    STAMP(12);
    BAR();
    LAS f32x4* red = (LAS f32x4*)(lds + K2_RED3);
#pragma unroll
    for (int r = 0; r < 2; ++r)
#pragma unroll
      for (int c = 0; c < 4; ++c) red[(((wm * 2 + r) * 4 + c) * 4 + wk) * 64 + lane] = acc[r][c];
    BAR();
    f32x4 o[2];
#pragma unroll
    for (int n = 0; n < 2; ++n) {
      o[n] = red[(((wm * 2 + er) * 4 + 2 * cg + n) * 4 + 0) * 64 + lane];
#pragma unroll
      for (int k2 = 1; k2 < 4; ++k2) o[n] += red[(((wm * 2 + er) * 4 + 2 * cg + n) * 4 + k2) * 64 + lane];
    }
    u32x4 res;
#pragma unroll
    for (int n = 0; n < 2; ++n)
#pragma unroll
      for (int e = 0; e < 2; ++e) {
        const unsigned xw = xv[2 * n + e], yw = ya[2 * n + e];
        const f32x2 xa = rb2((f32x2){bflo(xw) + bflo(yw), bfhi(xw) + bfhi(yw)}), ob = rb2((f32x2){o[n][2 * e], o[n][2 * e + 1]});
        res[2 * n + e] = pk2(xa[0] + ob[0], xa[1] + ob[1]);
      }
    *(u32x4*)(p.out + (size_t)t * 512 + d0) = res;
  }
  STAMP(13);
}

extern "C" __global__ void zz_text_end() {}

extern "C" void kernel_launch(void* const* d_in, const int* in_sizes, int n_in, void* d_out, int out_size, void* d_ws, size_t ws_size,
                              hipStream_t stream) {
  (void)in_sizes; (void)n_in; (void)out_size; (void)ws_size;
  Params p;
  p.x = (const u16*)d_in[0]; p.a_re = (const u16*)d_in[1]; p.a_im = (const u16*)d_in[2]; p.b_re = (const u16*)d_in[3];
  p.b_im = (const u16*)d_in[4]; p.c_re = (const u16*)d_in[5]; p.c_im = (const u16*)d_in[6]; p.log_dt = (const u16*)d_in[7];
  p.dskip = (const u16*)d_in[8]; p.w_in = (const u16*)d_in[10]; p.w_sp = (const u16*)d_in[11]; p.b_sp = (const u16*)d_in[12];
  p.w_out = (const u16*)d_in[13]; p.out = (u16*)d_out; p.ws = (char*)d_ws;
  (void)hipFuncSetAttribute((const void*)k1_kernel, hipFuncAttributeMaxDynamicSharedMemorySize, K1_LDS);
  (void)hipFuncSetAttribute((const void*)k2_kernel, hipFuncAttributeMaxDynamicSharedMemorySize, K2_LDS);
  k1_kernel<<<256, 768, K1_LDS, stream>>>(p);
  k2_kernel<<<256, 512, K2_LDS, stream>>>(p);
}
```

```cpp
#include <hip/hip_runtime.h>
#include <stdint.h>

typedef unsigned short u16;
typedef __bf16 bf16x8 __attribute__((ext_vector_type(8), may_alias));
typedef __bf16 bf16x2 __attribute__((ext_vector_type(2)));
typedef short s16x4 __attribute__((ext_vector_type(4), may_alias));
typedef short s16x8 __attribute__((ext_vector_type(8), may_alias));
typedef float f32x4 __attribute__((ext_vector_type(4), may_alias));
typedef float f32x2 __attribute__((ext_vector_type(2), may_alias));
typedef unsigned u32x4 __attribute__((ext_vector_type(4), may_alias));
typedef unsigned u32x2 __attribute__((ext_vector_type(2), may_alias));
#define LAS __attribute__((address_space(3)))
#define GAS __attribute__((address_space(1)))
typedef GAS unsigned long long gu64;
typedef GAS unsigned gu32;
#define RLX_AGENT __ATOMIC_RELAXED, __HIP_MEMORY_SCOPE_AGENT

constexpr size_t WS_XCH = 0;
constexpr size_t WS_EP = 512u << 10;
constexpr size_t WS_CNT = 576u << 10;
#define SIDE_PRIO 2
#define SIDE_PRIO2 SIDE_PRIO
#define SIDE_PRIO3 SIDE_PRIO
#define LOADER_PRIO 3
#define GEMM_PRIO 0
constexpr size_t WS_Z1F = 1u << 20;
constexpr size_t WS_Z2F = 5u << 20;
constexpr size_t WS_XNT = 9u << 20;
constexpr size_t WS_KG = 11u << 20;
constexpr size_t WS_HF = 14u << 20;
constexpr size_t WS_YA2 = 18u << 20;

struct Params {
  const u16* x; const u16* a_re; const u16* a_im; const u16* b_re; const u16* b_im; const u16* c_re; const u16* c_im;
  const u16* log_dt; const u16* dskip; const u16* w_in; const u16* w_sp; const u16* b_sp; const u16* w_out;
  u16* out; char* ws;
};

__device__ __forceinline__ float bf2f(unsigned h) { return __builtin_bit_cast(float, h << 16); }
__device__ __forceinline__ float bflo(unsigned w) { return __builtin_bit_cast(float, w << 16); }
__device__ __forceinline__ float bfhi(unsigned w) { return __builtin_bit_cast(float, w & 0xffff0000u); }
__device__ __forceinline__ unsigned pk2(float lo, float hi) { bf16x2 v = {(__bf16)lo, (__bf16)hi}; return __builtin_bit_cast(unsigned, v); }
__device__ __forceinline__ float rb(float v) { return (float)(__bf16)v; }
template <int CTRL> __device__ __forceinline__ float dpp_add(float v) {
  return v + __builtin_bit_cast(float, __builtin_amdgcn_update_dpp(0, __builtin_bit_cast(int, v), CTRL, 0xf, 0xf, true));
}
__device__ __forceinline__ float allred16(float v) { v = dpp_add<0xB1>(v); v = dpp_add<0x4E>(v); v = dpp_add<0x141>(v); v = dpp_add<0x140>(v); return v; }
__device__ __forceinline__ float gelu_tanh(float v) {
  const float t = v * v;
  const float u = v * __builtin_fmaf(t, 0.044715f, 1.0f);
  const float e = __builtin_amdgcn_exp2f(u * (-2.0f * 0.7978845608028654f * 1.4426950408889634f));
  return v * __builtin_amdgcn_rcpf(1.0f + e);
}
#define GLDS16(g, l) __builtin_amdgcn_global_load_lds((const unsigned*)(g), (LAS unsigned*)(l), 16, 0, 0)
#define GLDS4(g, l) __builtin_amdgcn_global_load_lds((const unsigned*)(g), (LAS unsigned*)(l), 4, 0, 0)
#define WAIT_V(n) asm volatile("s_waitcnt vmcnt(%0)" ::"i"(n) : "memory")
#define BAR() do { asm volatile("s_waitcnt lgkmcnt(0)" ::: "memory"); __builtin_amdgcn_s_barrier(); asm volatile("" ::: "memory"); } while (0)
__device__ __forceinline__ f32x4 mfma16(bf16x8 a, bf16x8 b, f32x4 c) { return __builtin_amdgcn_mfma_f32_16x16x32_bf16(a, b, c, 0, 0, 0); }
__device__ __forceinline__ s16x4 trread(LAS char* a) { return __builtin_amdgcn_ds_read_tr16_b64_v4i16((LAS s16x4*)a); }
#define CAS __attribute__((address_space(4)))
__device__ __forceinline__ unsigned ldsaddr(LAS char* a) { return (unsigned)(uintptr_t)a; }
#define GLOAD16(dst, voff, sbase, off) asm volatile("global_load_dwordx4 %0, %1, %2 offset:%3" : "=v"(dst) : "v"(voff), "s"(sbase), "i"(off) : "memory")
#define K1_AUX 0
#define WS_STORE16(off, v) __builtin_amdgcn_raw_buffer_store_b128((v), wrs, (int)(off), 0, K1_AUX)
#define WS_STORE8(off, v) __builtin_amdgcn_raw_buffer_store_b64((v), wrs, (int)(off), 0, K1_AUX)
#define TRREAD_ASM(dst, addr, off) asm volatile("ds_read_b64_tr_b16 %0, %1 offset:%2" : "=v"(dst) : "v"(addr), "i"(off) : "memory")
__device__ __forceinline__ bf16x8 cat8(s16x4 lo, s16x4 hi) { return __builtin_bit_cast(bf16x8, __builtin_shufflevector(lo, hi, 0, 1, 2, 3, 4, 5, 6, 7)); }

__device__ __forceinline__ const char* text_begin(unsigned& bytes) {
  unsigned long long pc; unsigned off, len;
  asm volatile("s_getpc_b64 %0\n.Lpc%=:\n\ts_mov_b32 %1, .Lpc%=-k1_kernel\n\ts_mov_b32 %2, zz_text_end-k1_kernel" : "=s"(pc), "=s"(off), "=s"(len));
  pc -= off;
  bytes = len;
  return (const char*)pc;
}

#define STAMP_DECL
#define STAMP(i)
#define STAMP_FLUSH(kern, n)

constexpr int K1_RING = 65536, K1_STG = 16384, K1_SCR = K1_RING + 3 * K1_STG  , K1_SCS = K1_SCR + 32768  ,
              K1_ST = K1_SCS + 1024, K1_S1 = K1_ST + 1024, K1_P = K1_S1 + 512, K1_S2 = K1_P + 2048, K1_PF = K1_S2 + 512, K1_LDS = K1_PF + 1024;

__device__ __forceinline__ f32x2 gelu2(f32x2 v) {
  constexpr float C1 = -2.0f * 0.7978845608028654f * 1.4426950408889634f, C2 = C1 * 0.044715f;
  const f32x2 t = v * v;
  const f32x2 u = v * (t * C2 + C1);
  f32x2 e; e[0] = __builtin_amdgcn_exp2f(u[0]); e[1] = __builtin_amdgcn_exp2f(u[1]);
  e = e + 1.0f;
  f32x2 r; r[0] = __builtin_amdgcn_rcpf(e[0]); r[1] = __builtin_amdgcn_rcpf(e[1]);
  return v * r;
}
__device__ __forceinline__ f32x2 rb2(f32x2 v) { const unsigned u = pk2(v[0], v[1]); return (f32x2){bflo(u), bfhi(u)}; }

extern "C" __global__ __launch_bounds__(768, 3) void k1_kernel(Params p) {
  extern __shared__ __attribute__((aligned(16))) char smem[];
  LAS char* lds = (LAS char*)smem;
  const int tid = threadIdx.x, lane = tid & 63, w = __builtin_amdgcn_readfirstlane(tid >> 6);
  const int b = blockIdx.x, xcd = b & 7, idx = b >> 3;
  const int fr = lane & 15, g = lane >> 4;
  if (idx >= 28) {
    if (w >= 8) return;
    const int sp = xcd * 4 + (idx - 28);
    const char* src = (const char*)p.w_sp + (size_t)(64 * sp + 8 * w) * 4096 + lane * 16;
#pragma unroll 4
    for (int n = 0; n < 32; ++n) GLDS16(src + n * 1024, lds + w * 8192 + (n & 7) * 1024);
    if (w < 7) {
      const char* so = (const char*)p.w_out + (size_t)(28 * sp + 4 * w) * 1024 + lane * 16;
#pragma unroll
      for (int n = 0; n < 4; ++n) GLDS16(so + n * 1024, lds + w * 8192 + n * 1024);
    }
    WAIT_V(0);
    return;
  }
  const int ib = 4 * xcd + (idx & 3), jb = idx >> 2;
  const int rot = ib & 7;
  STAMP_DECL
  STAMP(0);

  if (w >= 8) {
    const int lw = w - 8;
    unsigned tbytes;
    const char* cbase = text_begin(tbytes);
    if (lw == 3 && (unsigned)(idx + 1) * 1024u <= tbytes) GLDS16(cbase + idx * 1024 + lane * 16, lds + K1_PF);
    unsigned voff[4], xvoff[2];
#pragma unroll
    for (int nn = 0; nn < 4; ++nn) {
      const int row = 4 * (4 * lw + nn) + g;
      voff[nn] = (unsigned)(row * 1792) * 2u + (unsigned)(fr ^ ((g << 2) | nn)) * 16u;
    }
#pragma unroll
    for (int nn = 0; nn < 2; ++nn) {
      const int row = 16 * lw + 8 * nn + (lane >> 3);
      xvoff[nn] = (unsigned)row * 1024u + (unsigned)((lane & 7) ^ ((row >> 1) & 7)) * 16u;
    }
    auto ldS = [&](int s) {
      const int half = s >> 3, kst = ((s & 7) + rot) & 7;
      if (s < 8) {
        const char* xb = (const char*)p.x + (size_t)(64 * ib) * 1024 + kst * 128;
        LAS char* xd = lds + kst * 8192 + lw * 2048;
        GLDS16(xb + xvoff[0], xd); GLDS16(xb + xvoff[1], xd + 1024);
      }
      const char* base = (const char*)p.w_in + ((size_t)(64 * kst) * 1792 + (half == 0 ? 896 : 0) + 128 * jb) * 2;
      LAS char* dst = lds + K1_RING + (s % 3) * K1_STG + lw * 4096;
#pragma unroll
      for (int nn = 0; nn < 4; ++nn) GLDS16(base + voff[nn], dst + nn * 1024);
    };
    ldS(0); ldS(1);
#pragma unroll
    for (int s = 0; s < 16; ++s) {
      if (s + 1 < 8) WAIT_V(6); else if (s + 1 < 16) WAIT_V(4); else WAIT_V(0);
      BAR();
      if (s + 2 < 16) ldS(s + 2);
    }
    if (lw == 3) {
      for (unsigned o = (unsigned)(28 + idx) * 1024u; o + 1024u <= tbytes; o += 28u * 1024u) GLDS16(cbase + o + lane * 16, lds + K1_PF);
      WAIT_V(0);
    }
    BAR(); BAR(); BAR();
    return;
  }

  const int wn = w & 3, wk = w >> 2;
  const __amdgpu_buffer_rsrc_t wrs = __builtin_amdgcn_make_buffer_rsrc(p.ws, 0, 16u << 20, 0x00020000);
  const unsigned tag = *(const CAS unsigned*)(p.ws + WS_EP) + 1u;
  const bf16x8 ones = __builtin_bit_cast(bf16x8, (u32x4){0x3F803F80u, 0x3F803F80u, 0x3F803F80u, 0x3F803F80u});
  unsigned boff[2][2];
#pragma unroll
  for (int c = 0; c < 2; ++c)
#pragma unroll
    for (int t = 0; t < 2; ++t) {
      const int row = 32 * wk + 8 * g + 4 * t + (fr >> 2), chn = 2 * (2 * wn + c) + ((fr & 3) >> 1);
      boff[c][t] = K1_RING + 256 * row + 16 * (chn ^ (((fr >> 2) << 2) | ((2 * g + t) & 3))) + 8 * (fr & 1);
    }
  const unsigned aoff = (unsigned)(fr * 128 + (((4 * wk + g) ^ ((fr >> 1) & 7)) * 16));
  f32x4 acc[4][2], cs[2];
  f32x4 za[2][2];
  float zcs[2];
  f32x4 sa = {0, 0, 0, 0}, sq = {0, 0, 0, 0};
  LAS f32x4* scr = (LAS f32x4*)(lds + K1_SCR);
  LAS float* scs = (LAS float*)(lds + K1_SCS);
  struct Frag { bf16x8 a[4]; bf16x8 b[2]; bf16x8 sf; };
  Frag F0, F1;
  auto rd = [&](int s, Frag& F, bool stats) {
    LAS char* xa = lds + (((s & 7) + rot) & 7) * 8192 + aoff;
#pragma unroll
    for (int rt = 0; rt < 4; ++rt) F.a[rt] = *(LAS bf16x8*)(xa + rt * 2048);
    const int bo = (s % 3) * K1_STG;
#pragma unroll
    for (int c = 0; c < 2; ++c) F.b[c] = cat8(trread(lds + boff[c][0] + bo), trread(lds + boff[c][1] + bo));
    if (stats) F.sf = *(LAS bf16x8*)(xa + wn * 2048);
  };
  auto mm2 = [&](Frag& F) {
#pragma unroll
    for (int c = 0; c < 2; ++c) {
#pragma unroll
      for (int rt = 0; rt < 4; ++rt) acc[rt][c] = mfma16(F.a[rt], F.b[c], acc[rt][c]);
      cs[c] = mfma16(ones, F.b[c], cs[c]);
    }
    sa = mfma16(F.sf, ones, sa); sq = mfma16(F.sf, F.sf, sq);
  };
  auto mm1 = [&](Frag& F) {
#pragma unroll
    for (int c = 0; c < 2; ++c) {
#pragma unroll
      for (int rt = 0; rt < 4; ++rt) acc[rt][c] = mfma16(F.b[c], F.a[rt], acc[rt][c]);
      cs[c] = mfma16(F.b[c], ones, cs[c]);
    }
  };
  auto swap_keep = [&]() {
    if (wk) {
#pragma unroll
      for (int r = 0; r < 2; ++r)
#pragma unroll
        for (int c = 0; c < 2; ++c) { const f32x4 tmp = acc[r][c]; acc[r][c] = acc[2 + r][c]; acc[2 + r][c] = tmp; asm volatile("" : "+v"(acc[r][c]), "+v"(acc[2 + r][c])); }
    }
#pragma unroll
    for (int r = 0; r < 2; ++r)
#pragma unroll
      for (int c = 0; c < 2; ++c) scr[((wn * 2 + wk) * 4 + 2 * r + c) * 64 + lane] = acc[2 + r][c];
  };
#pragma unroll
  for (int rt = 0; rt < 4; ++rt)
#pragma unroll
    for (int c = 0; c < 2; ++c) acc[rt][c] = (f32x4){0, 0, 0, 0};
  cs[0] = (f32x4){0, 0, 0, 0}; cs[1] = (f32x4){0, 0, 0, 0};
  BAR();
  STAMP(1);
  rd(0, F0, true);
#pragma unroll 1
  for (int s = 0; s < 6; s += 2) {
    BAR(); rd(s + 1, F1, true); __builtin_amdgcn_sched_barrier(0); mm2(F0);
    BAR(); rd(s + 2, F0, true); __builtin_amdgcn_sched_barrier(0); mm2(F1);
  }
  BAR(); rd(7, F1, true); __builtin_amdgcn_sched_barrier(0); mm2(F0);
  BAR(); rd(8, F0, false); __builtin_amdgcn_sched_barrier(0); mm2(F1);
  STAMP(2);
  {
    LAS float* ST = (LAS float*)(lds + K1_ST);
    if (fr == 0) {
#pragma unroll
      for (int r = 0; r < 4; ++r) ST[(16 * wn + 4 * g + r) * 4 + 2 * wk] = sa[r];
    }
    const float dq = (fr & 2) ? ((fr & 1) ? sq[3] : sq[2]) : ((fr & 1) ? sq[1] : sq[0]);
    if ((fr >> 2) == g) ST[(16 * wn + fr) * 4 + 2 * wk + 1] = dq;
  }
  swap_keep();
  if (g == 0) { scs[((wn * 2 + wk) * 2 + 0) * 16 + fr] = cs[0][0]; scs[((wn * 2 + wk) * 2 + 1) * 16 + fr] = cs[1][0]; }
#pragma unroll
  for (int r = 0; r < 2; ++r)
#pragma unroll
    for (int c = 0; c < 2; ++c) za[r][c] = acc[r][c];
  zcs[0] = cs[0][0]; zcs[1] = cs[1][0];
#pragma unroll
  for (int rt = 0; rt < 4; ++rt)
#pragma unroll
    for (int c = 0; c < 2; ++c) acc[rt][c] = (f32x4){0, 0, 0, 0};
  cs[0] = (f32x4){0, 0, 0, 0}; cs[1] = (f32x4){0, 0, 0, 0};
  STAMP(3);
#pragma unroll
  for (int s = 8; s < 16; ++s) {
    Frag& Fc = (s & 1) ? F1 : F0;
    Frag& Fn = (s & 1) ? F0 : F1;
    f32x4 st = {0, 0, 0, 0};
    if (s < 15) BAR();
    if (s >= 9 && s < 13) st = *(LAS f32x4*)(lds + K1_S1 + (32 * wk + 16 * ((s - 9) >> 1) + 4 * g + 2 * ((s - 9) & 1)) * 8);
    if (s < 15) rd(s + 1, Fn, false);
    __builtin_amdgcn_sched_barrier(0);
    mm1(Fc);
    __builtin_amdgcn_sched_barrier(0);
    if (s == 8) {
#pragma unroll
      for (int r = 0; r < 2; ++r)
#pragma unroll
        for (int c = 0; c < 2; ++c) za[r][c] += scr[((wn * 2 + (wk ^ 1)) * 4 + 2 * r + c) * 64 + lane];
#pragma unroll
      for (int c = 0; c < 2; ++c) zcs[c] += scs[((wn * 2 + (wk ^ 1)) * 2 + c) * 16 + fr];
      if (w == 0) {
        const f32x4 v = *(LAS f32x4*)(lds + K1_ST + lane * 16);
        const float mean = (v[0] + v[2]) * (1.0f / 512.0f);
        const float var = __builtin_fmaxf((v[1] + v[3]) * (1.0f / 512.0f) - mean * mean, 0.f);
        const float den = rb(__builtin_sqrtf(rb(rb(var) + 1e-5f)));
        *(LAS f32x2*)(lds + K1_S1 + lane * 8) = (f32x2){rb(mean), __builtin_amdgcn_rcpf(den)};
      }
    }
    if (s >= 9 && s < 13) {
      const int rp = (s - 9) >> 1, r0 = 2 * ((s - 9) & 1);
      const int row = 32 * wk + 16 * rp + 4 * g + r0;
      float sm[2], sq2[2];
#pragma unroll
      for (int q = 0; q < 2; ++q) {
        f32x2 v = {za[rp][0][r0 + q], za[rp][1][r0 + q]};
        v = (v - st[2 * q] * (f32x2){zcs[0], zcs[1]}) * st[2 * q + 1];
        v = rb2(gelu2(v));
        za[rp][0][r0 + q] = v[0]; za[rp][1][r0 + q] = v[1];
        sm[q] = v[0] + v[1]; sq2[q] = v[0] * v[0] + v[1] * v[1];
      }
      sm[0] = dpp_add<0xB1>(sm[0]); sm[1] = dpp_add<0xB1>(sm[1]); sq2[0] = dpp_add<0xB1>(sq2[0]); sq2[1] = dpp_add<0xB1>(sq2[1]);
      sm[0] = dpp_add<0x4E>(sm[0]); sm[1] = dpp_add<0x4E>(sm[1]); sq2[0] = dpp_add<0x4E>(sq2[0]); sq2[1] = dpp_add<0x4E>(sq2[1]);
      sm[0] = dpp_add<0x141>(sm[0]); sm[1] = dpp_add<0x141>(sm[1]); sq2[0] = dpp_add<0x141>(sq2[0]); sq2[1] = dpp_add<0x141>(sq2[1]);
      sm[0] = dpp_add<0x140>(sm[0]); sm[1] = dpp_add<0x140>(sm[1]); sq2[0] = dpp_add<0x140>(sq2[0]); sq2[1] = dpp_add<0x140>(sq2[1]);
      if (fr == 0) {
        *(LAS f32x2*)(lds + K1_P + (row * 4 + wn) * 8) = (f32x2){sm[0], sq2[0]};
        *(LAS f32x2*)(lds + K1_P + ((row + 1) * 4 + wn) * 8) = (f32x2){sm[1], sq2[1]};
      }
    }
    if (s == 13 && w == 0) {
      const f32x4 pa = *(LAS f32x4*)(lds + K1_P + lane * 32), pc = *(LAS f32x4*)(lds + K1_P + lane * 32 + 16);
      const float m0 = pa[0] * (1.0f / 32.0f), m1 = pa[2] * (1.0f / 32.0f), m2_ = pc[0] * (1.0f / 32.0f), m3 = pc[2] * (1.0f / 32.0f);
      const float mj = (m0 + m1 + m2_ + m3) * 0.25f;
      const float w2 = __builtin_fmaxf(pa[1] - m0 * pa[0], 0.f) + __builtin_fmaxf(pa[3] - m1 * pa[2], 0.f) + __builtin_fmaxf(pc[1] - m2_ * pc[0], 0.f) + __builtin_fmaxf(pc[3] - m3 * pc[2], 0.f);
      const float da = m0 - mj, db = m1 - mj, dc = m2_ - mj, dd = m3 - mj;
      const float m2 = w2 + 32.0f * ((da * da + db * db) + (dc * dc + dd * dd));
      gu64* slot = (gu64*)(p.ws + WS_XCH) + ((size_t)(ib * 7 + jb) * 64 + lane) * 2;
      __hip_atomic_store(slot, ((unsigned long long)tag << 32) | __builtin_bit_cast(unsigned, mj), RLX_AGENT);
      __hip_atomic_store(slot + 1, ((unsigned long long)tag << 32) | __builtin_bit_cast(unsigned, m2), RLX_AGENT);
    }
    if (s == 14) {
      const int nblk = jb < 4 ? 5 : 4;
      for (int u = w; u < 2 * nblk; u += 8) {
        const int dblk = jb + 7 * (u >> 1), t0 = 32 * (u & 1) + 8 * g;
        const int chunk = 2 * (dblk & 3) + ((fr & 3) >> 1);
        s16x4 h[2];
#pragma unroll
        for (int tt = 0; tt < 2; ++tt) {
          const int row = t0 + 4 * tt + (fr >> 2);
          h[tt] = trread(lds + (dblk >> 2) * 8192 + row * 128 + ((chunk ^ ((row >> 1) & 7)) * 16) + 8 * (fr & 1));
        }
        const u32x2 h0 = __builtin_bit_cast(u32x2, h[0]), h1 = __builtin_bit_cast(u32x2, h[1]);
        u32x4 o;
#pragma unroll
        for (int e = 0; e < 4; ++e) {
          const f32x4 st = *(LAS f32x4*)(lds + K1_S1 + (t0 + 2 * e) * 8);
          const unsigned xw = e < 2 ? h0[e] : h1[e - 2];
          const f32x2 dv = rb2((f32x2){bflo(xw) - st[0], bfhi(xw) - st[2]});
          o[e] = pk2(dv[0] * st[1], dv[1] * st[3]);
        }
        WS_STORE16(WS_XNT + ((16 * dblk + fr) * 2048 + 64 * ib + t0) * 2, o);
      }
    }
  }
  STAMP(4);
  BAR();
  swap_keep();
  if (fr == 0) {
#pragma unroll
    for (int c = 0; c < 2; ++c)
#pragma unroll
      for (int r = 0; r < 4; ++r) scs[((wn * 2 + wk) * 2 + c) * 16 + 4 * g + r] = cs[c][r];
  }
  BAR();
  STAMP(5);
#pragma unroll
  for (int rp = 0; rp < 2; ++rp) {
    const f32x2 st = *(LAS f32x2*)(lds + K1_S1 + (32 * wk + 16 * rp + fr) * 8);
    u32x4 o;
#pragma unroll
    for (int c = 0; c < 2; ++c) {
      const f32x4 v = acc[rp][c] + scr[((wn * 2 + (wk ^ 1)) * 4 + 2 * rp + c) * 64 + lane];
      const f32x4 cc = cs[c] + *(LAS f32x4*)(scs + ((wn * 2 + (wk ^ 1)) * 2 + c) * 16 + 4 * g);
#pragma unroll
      for (int h = 0; h < 2; ++h) {
        f32x2 z = {v[2 * h], v[2 * h + 1]};
        z = (z - st[0] * (f32x2){cc[2 * h], cc[2 * h + 1]}) * st[1];
        z = gelu2(z);
        o[2 * c + h] = pk2(z[0], z[1]);
      }
    }
    const int t16 = 4 * ib + 2 * wk + rp, cp = 4 * jb + wn;
    WS_STORE16(WS_Z1F + ((t16 * 28 + cp) * 64 + lane) * 16, o);
  }
  STAMP(6);
  if (w == 0) {
    gu64* slot = (gu64*)(p.ws + WS_XCH) + ((size_t)(ib * 7) * 64 + lane) * 2;
    float mj[7], m2j[7];
    for (unsigned spins = 0; spins < (1u << 22); ++spins) {
      bool ok = true;
#pragma unroll
      for (int j = 0; j < 7; ++j) {
        const unsigned long long u0 = __hip_atomic_load(slot + j * 128, RLX_AGENT), u1 = __hip_atomic_load(slot + j * 128 + 1, RLX_AGENT);
        mj[j] = __builtin_bit_cast(float, (unsigned)u0); m2j[j] = __builtin_bit_cast(float, (unsigned)u1);
        ok &= ((unsigned)(u0 >> 32) == tag) & ((unsigned)(u1 >> 32) == tag);
      }
      if (__all(ok)) break;
      __builtin_amdgcn_s_sleep(1);
    }
    float mean = 0.f;
#pragma unroll
    for (int j = 0; j < 7; ++j) mean += mj[j];
    mean *= (1.0f / 7.0f);
    float m2 = 0.f;
#pragma unroll
    for (int j = 0; j < 7; ++j) { const float dm = mj[j] - mean; m2 += m2j[j] + 128.0f * dm * dm; }
    const float var = m2 * (1.0f / 896.0f);
    const float den = rb(__builtin_sqrtf(rb(rb(var) + 1e-5f)));
    *(LAS f32x2*)(lds + K1_S2 + lane * 8) = (f32x2){rb(mean), __builtin_amdgcn_rcpf(den)};
  }
  BAR();
  STAMP(7);
  {
#pragma unroll
    for (int c = 0; c < 2; ++c) {
      u32x4 o;
#pragma unroll
      for (int rp = 0; rp < 2; ++rp) {
        const f32x4 s01 = *(LAS f32x4*)(lds + K1_S2 + (32 * wk + 16 * rp + 4 * g) * 8), s23 = *(LAS f32x4*)(lds + K1_S2 + (32 * wk + 16 * rp + 4 * g + 2) * 8);
        const f32x2 d0 = {za[rp][c][0] - s01[0], za[rp][c][1] - s01[2]}, d1 = {za[rp][c][2] - s23[0], za[rp][c][3] - s23[2]};
        o[2 * rp] = pk2(d0[0] * s01[1], d0[1] * s01[3]); o[2 * rp + 1] = pk2(d1[0] * s23[1], d1[1] * s23[3]);
      }
      const int ks = 2 * ib + wk, c16 = 8 * jb + 2 * wn + c;
      const int zf = (int)WS_Z2F + (ks * 56 + c16) * 1024 + (fr + 16 * (g >> 1)) * 16 + (g & 1) * 8;
      WS_STORE8(zf, ((u32x2){o[0], o[1]}));
      WS_STORE8(zf + 512, ((u32x2){o[2], o[3]}));
    }
  }
  if (jb == 0 && w == 1 && lane < 9) *(unsigned*)(p.ws + WS_CNT + (lane < 8 ? ib * 8 + lane : 256 + (ib & 7)) * 128) = 0u;
  if (b == 0 && w == 0) {
    gu64* slot = (gu64*)(p.ws + WS_XCH);
    for (unsigned spins = 0; spins < (1u << 22); ++spins) {
      bool ok = true;
#pragma unroll
      for (int q = 0; q < 4; ++q) {
        const int ij = lane + 64 * q;
        if (ij < 224) ok &= ((unsigned)(__hip_atomic_load(slot + (size_t)ij * 128, RLX_AGENT) >> 32) == tag);
      }
      if (__all(ok)) break;
      __builtin_amdgcn_s_sleep(1);
    }
    if (lane == 0) __hip_atomic_store((gu32*)(p.ws + WS_EP), tag, RLX_AGENT);
  }
  STAMP(8);
  STAMP_FLUSH(0, 9);
}

constexpr int K2_A = 0  , K2_TAB = 65536, K2_TABW = 64 * 272  , K2_G = K2_TAB + 4 * K2_TABW  , K2_U = K2_G + 2 * 4128  ,
              K2_RCS = 4160, K2_RCCH = 4 * K2_RCS  , K2_PART = K2_TAB + 2 * K2_RCCH  ,
              K2_FLG = 163840 - 64, K2_LDS = 163840;
constexpr int K2_RED3 = 0;

__device__ __forceinline__ f32x2 cmul(f32x2 a, f32x2 b) {
  const f32x2 t = (f32x2){a[0], a[0]} * b;
  return (f32x2){a[1], a[1]} * (f32x2){-b[1], b[0]} + t;
}
__device__ __forceinline__ void wait_flags(LAS char* lds, int quad, int step, int mask) {
  LAS int* flg = (LAS int*)(lds + K2_FLG) + 4 * quad;
  for (;;) {
    const u32x4 v = *(volatile LAS u32x4*)flg;
    bool ok = true;
#pragma unroll
    for (int i = 0; i < 4; ++i) ok &= (!((mask >> i) & 1)) || ((int)v[i] >= step);
    if (__builtin_amdgcn_readfirstlane(ok)) break;
    __builtin_amdgcn_s_sleep(1);
  }
  asm volatile("" ::: "memory");
}
__device__ __forceinline__ void set_flag(LAS char* lds, int w, int step, int lane) {
  asm volatile("s_waitcnt lgkmcnt(0)" ::: "memory");
  if (lane == 0) __hip_atomic_store((LAS int*)(lds + K2_FLG) + w, step, __ATOMIC_RELAXED, __HIP_MEMORY_SCOPE_WORKGROUP);
}
__device__ __forceinline__ void group_sync(LAS char* lds, int w, int step, int mask, int lane) { set_flag(lds, w, step, lane); wait_flags(lds, w >> 2, step, mask); }
__device__ __forceinline__ LAS char* wpiece(LAS char* lds, int piece) { return lds + piece * 1024; }
__device__ __forceinline__ void poll_ge(gu32* word, unsigned want) {
  for (unsigned spins = 0; spins < (1u << 22); ++spins) {
    if ((unsigned)__builtin_amdgcn_readfirstlane(__hip_atomic_load(word, RLX_AGENT)) >= want) break;
    __builtin_amdgcn_s_sleep(1);
  }
  asm volatile("" ::: "memory");
}

template <bool V> struct BoolC { static constexpr bool value = V; };
template <int V> struct IntC { static constexpr int value = V; };
template <int NC>
__device__ __forceinline__ void gemm2_wave(const Params& p, LAS char* lds, int w, int lane, int ib, int jb, __amdgpu_buffer_rsrc_t hrs) {
  const int fr = lane & 15, g = lane >> 4, cw = w & 3, kh = w >> 2, c0 = 2 * cw;
  typedef BoolC<false> F_;
  typedef BoolC<true> T_;
  f32x4 acc[4][NC];
#pragma unroll
  for (int r = 0; r < 4; ++r)
#pragma unroll
    for (int c = 0; c < NC; ++c) acc[r][c] = (f32x4){0, 0, 0, 0};
  const char* gB = p.ws + WS_Z2F + (size_t)(56 * kh + 7 * jb + c0) * 1024;
  const unsigned lane16 = lane * 16;
  u32x4 bq[4][NC];
  auto issueB = [&](int gq, u32x4 (&dst)[NC]) {
    const char* base = gB + (size_t)gq * (112 * 1024);
#pragma unroll
    for (int c = 0; c < NC; ++c) GLOAD16(dst[c], lane16, base, c * 1024);
  };
  LAS char* afr = lds + K2_A + fr * 128 + (((4 * kh + g) ^ ((fr >> 1) & 7)) * 16);
#pragma unroll
  for (int u = 0; u < 4; ++u) issueB(u, bq[u]);
  bf16x8 af[2][4];
  auto rdA = [&](int gq, bf16x8 (&dst)[4]) {
    LAS char* src = afr + (gq & 7) * 8192;
#pragma unroll
    for (int rt = 0; rt < 4; ++rt) dst[rt] = *(LAS bf16x8*)(src + rt * 2048);
  };
  LAS int* lflg = (LAS int*)(lds + K2_FLG) + 8;
  wait_flags(lds, 2, 1, 1);
  rdA(0, af[0]);
  u32x4 flv = *(volatile LAS u32x4*)lflg;
  auto step = [&](int gq, auto uc, auto last) {
    constexpr int u = decltype(uc)::value;
    constexpr bool fin = decltype(last)::value && u == 3;
    if (!fin) {
      constexpr int li = (u + 1) & 3;
      if (__builtin_amdgcn_readfirstlane((int)flv[li]) < gq + 2) wait_flags(lds, 2, gq + 2, 1 << li);
      *(volatile LAS int*)(lds + K2_FLG + w * 4) = gq + 1;
      rdA(gq + 1, af[(u + 1) & 1]);
      flv = *(volatile LAS u32x4*)lflg;
    }
    u32x4 (&bc)[NC] = bq[u];
    if constexpr (NC == 2) asm volatile("s_waitcnt vmcnt(%2)" : "+v"(bc[0]), "+v"(bc[1]) : "i"(2 * (decltype(last)::value ? 3 - u : 3)) : "memory");
    else asm volatile("s_waitcnt vmcnt(%1)" : "+v"(bc[0]) : "i"(decltype(last)::value ? 3 - u : 3) : "memory");
#pragma unroll
    for (int rt = 0; rt < 4; ++rt)
#pragma unroll
      for (int c = 0; c < NC; ++c) acc[rt][c] = mfma16(__builtin_bit_cast(bf16x8, bc[c]), af[u & 1][rt], acc[rt][c]);
    if constexpr (NC == 2) asm volatile("" : "+v"(acc[0][0]), "+v"(acc[0][1]), "+v"(acc[1][0]), "+v"(acc[1][1]), "+v"(acc[2][0]), "+v"(acc[2][1]), "+v"(acc[3][0]), "+v"(acc[3][1]));
    else asm volatile("" : "+v"(acc[0][0]), "+v"(acc[1][0]), "+v"(acc[2][0]), "+v"(acc[3][0]));
    __builtin_amdgcn_sched_barrier(0);
    if (!decltype(last)::value) issueB(gq + 4, bc);
  };
#pragma unroll 1
  for (int q = 0; q < 7; ++q) { step(4 * q, IntC<0>{}, F_{}); step(4 * q + 1, IntC<1>{}, F_{}); step(4 * q + 2, IntC<2>{}, F_{}); step(4 * q + 3, IntC<3>{}, F_{}); }
  u32x4 z1v[2]; u16 bsv[2];
#pragma unroll
  for (int r = 0; r < 2; ++r) {
    const int c16 = 7 * jb + c0;
    const char* zb = p.ws + WS_Z1F + (size_t)((4 * ib + 2 * kh + r) * 28) * 1024 + lane * 16;
    const u32x2 za = *(const u32x2*)(zb + (size_t)(c16 >> 1) * 1024 + (c16 & 1) * 8);
    u32x2 zb2 = {0, 0};
    if (NC == 2) zb2 = *(const u32x2*)(zb + (size_t)((c16 + 1) >> 1) * 1024 + ((c16 + 1) & 1) * 8);
    z1v[r] = (u32x4){za[0], za[1], zb2[0], zb2[1]};
    bsv[r] = p.b_sp[64 * ib + 16 * (2 * kh + r) + fr];
  }
  step(28, IntC<0>{}, T_{}); step(29, IntC<1>{}, T_{}); step(30, IntC<2>{}, T_{}); step(31, IntC<3>{}, T_{});
  set_flag(lds, w, 33, lane);
  wait_flags(lds, 0, 33, 0xf); wait_flags(lds, 1, 33, 0xf);
  if (kh) {
#pragma unroll
    for (int r = 0; r < 2; ++r)
#pragma unroll
      for (int c = 0; c < NC; ++c) { const f32x4 tmp = acc[r][c]; acc[r][c] = acc[2 + r][c]; acc[2 + r][c] = tmp; asm volatile("" : "+v"(acc[r][c]), "+v"(acc[2 + r][c])); }
  }
  LAS f32x4* xch = (LAS f32x4*)(lds + K2_A);
#pragma unroll
  for (int r = 0; r < 2; ++r)
#pragma unroll
    for (int c = 0; c < NC; ++c) xch[(w * 4 + 2 * r + c) * 64 + lane] = acc[2 + r][c];
  set_flag(lds, w, 34, lane);
  wait_flags(lds, kh ^ 1, 34, 1 << cw);
#pragma unroll
  for (int r = 0; r < 2; ++r) {
    const float bs = bf2f(bsv[r]);
    unsigned hw[4] = {0, 0, 0, 0};
#pragma unroll
    for (int c = 0; c < NC; ++c) {
      const f32x4 v = acc[r][c] + xch[((w ^ 4) * 4 + 2 * r + c) * 64 + lane];
      const f32x2 g01 = rb2(rb2((f32x2){v[0], v[1]}) + bs), g23 = rb2(rb2((f32x2){v[2], v[3]}) + bs);
      hw[2 * c] = pk2(bflo(z1v[r][2 * c]) * g01[0], bfhi(z1v[r][2 * c]) * g01[1]);
      hw[2 * c + 1] = pk2(bflo(z1v[r][2 * c + 1]) * g23[0], bfhi(z1v[r][2 * c + 1]) * g23[1]);
    }
    const int hb = ((ib * 8 + jb) * 4 + 2 * kh + r) * 3584 + cw * 1024;
    if (NC == 2) __builtin_amdgcn_raw_buffer_store_b128((u32x4){hw[0], hw[1], hw[2], hw[3]}, hrs, hb + lane * 16, 0, 16);
    else __builtin_amdgcn_raw_buffer_store_b64((u32x2){hw[0], hw[1]}, hrs, hb + lane * 8, 0, 16);
  }
  WAIT_V(0);
}

extern "C" __global__ __launch_bounds__(1024, 4) void k2_kernel(Params p) {
  extern __shared__ __attribute__((aligned(16))) char smem[];
  LAS char* lds = (LAS char*)smem;
  const int tid = threadIdx.x, lane = tid & 63, w = __builtin_amdgcn_readfirstlane(tid >> 6);
  const int b = blockIdx.x, xcd = b & 7, idx = b >> 3;
  const int ib = (xcd >> 1) * 8 + (idx & 7), jb = (xcd & 1) * 4 + (idx >> 3);
  const int fr = lane & 15, g = lane >> 4;
  STAMP_DECL
  STAMP(0);
  if (tid < 16) *(LAS int*)(lds + K2_FLG + tid * 4) = 0;
  u16 prm[6] = {0, 0, 0, 0, 0, 0};
  u32x4 u0 = {0, 0, 0, 0}, u1 = {0, 0, 0, 0};
  if (w >= 12) {
    const int sw = w - 12, d = 2 * b + (sw >> 1), n = 64 * (sw & 1) + lane;
    prm[0] = p.a_re[d * 128 + n]; prm[1] = p.a_im[d * 128 + n]; prm[2] = p.b_re[d * 128 + n]; prm[3] = p.b_im[d * 128 + n];
    prm[4] = p.c_re[d * 128 + n]; prm[5] = p.c_im[d * 128 + n];
    u0 = *(const u32x4*)(p.ws + WS_XNT + ((size_t)d * 2048 + 1024 * (sw & 1)) * 2 + lane * 16);
    u1 = *(const u32x4*)(p.ws + WS_XNT + ((size_t)d * 2048 + 1024 * (sw & 1) + 512) * 2 + lane * 16);
  }
  BAR();
  const __amdgpu_buffer_rsrc_t hrs = __builtin_amdgcn_make_buffer_rsrc(p.ws + WS_HF, 0, 32 * 8 * 4 * 3584, 0x00020000);
  const __amdgpu_buffer_rsrc_t yrs = __builtin_amdgcn_make_buffer_rsrc(p.ws + WS_YA2, 0, 256 * 8192, 0x00020000);
  gu32* cnt = (gu32*)(p.ws + WS_CNT);
  const int r8w = lane >> 3;
  const char* wsrc = (const char*)p.w_out + (size_t)r8w * 1024 + jb * 128 + (((lane & 7) ^ (2 * ((r8w >> 1) & 3))) * 16);
  if (w < 8) {
    __builtin_amdgcn_s_setprio(GEMM_PRIO);
    if ((w & 3) < 3) gemm2_wave<2>(p, lds, w, lane, ib, jb, hrs); else gemm2_wave<1>(p, lds, w, lane, ib, jb, hrs);
    STAMP(1);
    set_flag(lds, w, 40, lane);
    wait_flags(lds, 0, 40, 0xf); wait_flags(lds, 1, 40, 0xf);
    if (tid == 0) __hip_atomic_fetch_add(cnt + (ib * 8 + jb) * 32, 1u, RLX_AGENT);
#pragma unroll
    for (int n = 0; n < 8; ++n) { const int pc = w * 8 + n; GLDS16(wsrc + (size_t)pc * 8192, wpiece(lds, pc)); }
    STAMP(2);
  } else if (w < 12) {
    __builtin_amdgcn_s_setprio(LOADER_PRIO);
    const int lw = w - 8;
    unsigned voffA[2];
#pragma unroll
    for (int par = 0; par < 2; ++par) voffA[par] = (unsigned)r8w * 4096u + (unsigned)(((lane & 7) ^ (((r8w >> 1) + 4 * par) & 7)) * 16);
    const char* gA = (const char*)p.w_sp + (size_t)(64 * ib) * 4096;
    auto ldA = [&](int gq) {
      LAS char* dst = lds + K2_A + (gq & 7) * 8192;
#pragma unroll
      for (int n = 0; n < 8; ++n) GLDS16(gA + gq * 128 + n * 32768 + voffA[n & 1], dst + n * 1024);
    };
#pragma unroll 1
    for (int gq = lw; gq < 32; gq += 4) {
      if (gq >= 8) { wait_flags(lds, 0, gq - 7, 0xf); wait_flags(lds, 1, gq - 7, 0xf); }
      ldA(gq);
      WAIT_V(0);
      set_flag(lds, w, gq + 1, lane);
    }
    __builtin_amdgcn_s_setprio(0);
  } else {
    __builtin_amdgcn_s_setprio(SIDE_PRIO);
    const int sw = w - 12, ch = sw >> 1, hh = sw & 1;
    const int pm = 3 << (2 * ch);
    const float are = bf2f(prm[0]), aim = bf2f(prm[1]), bre = bf2f(prm[2]), bim = bf2f(prm[3]), cre = bf2f(prm[4]), cim = bf2f(prm[5]);
    const unsigned ld2 = *(const CAS unsigned*)(p.log_dt + 2 * b);
    const unsigned dk2 = *(const CAS unsigned*)(p.dskip + 2 * b);
    const float dt = rb(expf(ch ? bfhi(ld2) : bflo(ld2)));
    LAS char* tab = lds + K2_TAB + sw * K2_TABW;
    STAMP(1);
    {
      const float lr = dt * are * 1.4426950408889634f, fq = dt * aim * 0.15915494309189535f;
      const float dbr = dt * bre, dbi = dt * bim;
      const f32x2 W = {cre * dbr - cim * dbi, cre * dbi + cim * dbr};
      auto cx = [&](float a) {
        const float m = __builtin_amdgcn_exp2f(lr * a), ph = __builtin_amdgcn_fractf(fq * a);
        return (f32x2){m * __builtin_amdgcn_cosf(ph), m * __builtin_amdgcn_sinf(ph)};
      };
      auto put = [&](int row, f32x2 v) { *(LAS unsigned*)(tab + row * 272 + lane * 4) = pk2(v[0], v[1]); };
      const f32x2 E1 = cx(1.f), E8 = cx(8.f), E128 = cx(128.f);
      f32x2 P[8];
      P[0] = (f32x2){1.f, 0.f}; P[1] = E1;
#pragma unroll
      for (int r = 2; r < 8; ++r) P[r] = cmul(P[r - 1], E1);
#pragma unroll
      for (int r = 0; r < 8; ++r) { put(r, P[r]); put(8 + r, cmul(P[r], E128)); }
      f32x2 F = cmul(E128, E128);
#pragma unroll
      for (int m = 0; m < 2; ++m) {
#pragma unroll
        for (int r = 0; r < 4; ++r) put(16 + 4 * m + r, r == 0 ? F : cmul(F, P[2 * r]));
        F = cmul(F, E128);
      }
#pragma unroll
      for (int m = 0; m < 4; ++m) {
        put(24 + 2 * m, F); put(25 + 2 * m, cmul(F, P[4]));
        F = cmul(F, E128);
      }
#pragma unroll
      for (int m = 0; m < 9; ++m) {
        put(32 + m, F);
        if (m < 8) F = cmul(F, E128);
      }
      f32x2 Y = W;
#pragma unroll
      for (int c = 0; c < 16; ++c) {
        put(48 + c, (f32x2){Y[0], -Y[1]});
        if (c < 15) Y = cmul(Y, E8);
      }
    }
    *(LAS u32x4*)(lds + K2_U + ch * 4608 + 512 + hh * 2048 + lane * 16) = u0;
    *(LAS u32x4*)(lds + K2_U + ch * 4608 + 512 + hh * 2048 + 1024 + lane * 16) = u1;
    if (hh == 0 && lane < 32) *(LAS u32x4*)(lds + K2_U + ch * 4608 + lane * 16) = (u32x4){0, 0, 0, 0};
    asm volatile("" ::: "memory");
    STAMP(2);
    f32x4 ka[3] = {{0, 0, 0, 0}, {0, 0, 0, 0}, {0, 0, 0, 0}};
#pragma unroll
    for (int ks = 0; ks < 4; ++ks) {
      const bf16x8 yf = *(LAS bf16x8*)(tab + (48 + fr) * 272 + ks * 64 + g * 16);
#pragma unroll
      for (int t = 0; t < 3; ++t) ka[t] = mfma16(*(LAS bf16x8*)(tab + (16 * t + fr) * 272 + ks * 64 + g * 16), yf, ka[t]);
    }
    asm volatile("" ::: "memory");
    if (hh == 0) *(LAS f32x4*)(tab + lane * 16) = ka[1];
    else { *(LAS f32x4*)(tab + lane * 16) = ka[0]; *(LAS f32x4*)(tab + 1024 + lane * 16) = ka[2]; }
    group_sync(lds, w, 1, pm, lane);
    STAMP(3);
    LAS char* T = lds + K2_TAB + (2 * ch) * K2_TABW + 4096;
    {
      LAS char* oth = lds + K2_TAB + (sw ^ 1) * K2_TABW;
      if (hh == 0) {
        const f32x4 s0 = ka[0] + *(LAS f32x4*)(oth + lane * 16), s2 = ka[2] + *(LAS f32x4*)(oth + 1024 + lane * 16);
        LAS u16* t0 = (LAS u16*)T + 4 * (g & 1) + 128 * (g >> 1) + 8 * fr;
#pragma unroll
        for (int r = 0; r < 4; ++r) t0[r] = (u16)(pk2(s0[r], 0.f) & 0xffffu);
        LAS u16* t2 = (LAS u16*)T + 1024 + 512 * g + 8 * fr;
        if (g < 2) {
#pragma unroll
          for (int r = 0; r < 4; ++r) t2[128 * r] = (u16)(pk2(s2[r], 0.f) & 0xffffu);
        } else if (g == 2) t2[0] = (u16)(pk2(s2[0], 0.f) & 0xffffu);
      } else {
        const f32x4 s1 = ka[1] + *(LAS f32x4*)(oth + lane * 16);
        LAS u16* t1 = (LAS u16*)T + (g < 2 ? 256 + 128 * g : 512 + 256 * (g - 2)) + 8 * fr;
        const int o1 = g < 2 ? 2 : 4, o2 = g < 2 ? 4 : 128, o3 = g < 2 ? 6 : 132;
        t1[0] = (u16)(pk2(s1[0], 0.f) & 0xffffu); t1[o1] = (u16)(pk2(s1[1], 0.f) & 0xffffu);
        t1[o2] = (u16)(pk2(s1[2], 0.f) & 0xffffu); t1[o3] = (u16)(pk2(s1[3], 0.f) & 0xffffu);
      }
    }
    group_sync(lds, w, 2, pm, lane);
    STAMP(4);
    LAS char* G = lds + K2_G + ch * 4128;
#pragma unroll
    for (int it = 0; it < 2; ++it) {
      const int gc = 128 * hh + 64 * it + lane;
      u32x4 o;
#pragma unroll
      for (int e = 0; e < 8; e += 2) {
        const int l0 = 2047 - (8 * gc + e), l1 = l0 - 1;
        const unsigned cv = pk2((float)l0, (float)l1);
        const int i0 = (int)bflo(cv), i1 = (int)bfhi(cv);
        o[e >> 1] = (unsigned)((LAS u16*)T)[i0] | ((unsigned)((LAS u16*)T)[i1] << 16);
      }
      *(LAS u32x4*)(G + gc * 16) = o;
    }
    if (hh == 0 && lane == 0) *(LAS u32x4*)(G + 4096) = (u32x4){0, 0, 0, 0};
    group_sync(lds, w, 3, 0xf, lane);
    STAMP(5);
    LAS char* rc = lds + K2_TAB + ch * K2_RCCH;
#pragma unroll
    for (int it = 0; it < 2; ++it) {
      const int gc = 128 * hh + 64 * it + lane;
      const u32x4 g0 = *(LAS u32x4*)(G + gc * 16), g1 = *(LAS u32x4*)(G + gc * 16 + 16);
      const unsigned wd[8] = {g0[0], g0[1], g0[2], g0[3], g1[0], g1[1], g1[2], g1[3]};
#pragma unroll
      for (int c = 0; c < 4; ++c) {
        const int s = 3 - c;
        u32x4 o;
#pragma unroll
        for (int i = 0; i < 4; ++i) o[i] = (s & 1) ? __builtin_amdgcn_alignbit(wd[(s >> 1) + i + 1], wd[(s >> 1) + i], 16) : wd[(s >> 1) + i];
        *(LAS u32x4*)(rc + c * K2_RCS + gc * 16) = o;
      }
    }
    if (hh == 0 && lane < 8) *(LAS u32x4*)(rc + (lane & 3) * K2_RCS + 4096 + (lane >> 2) * 16) = (u32x4){0, 0, 0, 0};
    group_sync(lds, w, 4, pm, lane);
    STAMP(6);
    __builtin_amdgcn_s_setprio(SIDE_PRIO2);
    f32x4 cacc[8];
#pragma unroll
    for (int r = 0; r < 8; ++r) cacc[r] = (f32x4){0, 0, 0, 0};
    LAS char* ub = lds + K2_U + ch * 4608 + (240 + 16 * fr + 8 * g) * 2;
    LAS char* kb = rc + (fr & 3) * K2_RCS + (2028 + 8 * g - 4 * (fr >> 2)) * 2;
#pragma unroll 1
    for (int pp = 0; pp < 4; ++pp) {
      const int p0 = 4 * hh + pp;
      bf16x8 ua[8], kf[8];
#pragma unroll
      for (int r = 0; r < 8; ++r) {
        LAS char* ka_ = kb - (p0 + 8 * r) * 64;
        kf[r] = cat8(*(LAS s16x4*)ka_, *(LAS s16x4*)(ka_ + 8));
        ua[r] = *(LAS bf16x8*)(ub + (8 * r - p0) * 64);
      }
      __builtin_amdgcn_sched_barrier(0);
#pragma unroll
      for (int i = 0; i < 8; ++i)
#pragma unroll
        for (int rt = i; rt < 8; ++rt) cacc[rt] = mfma16(ua[rt - i], kf[i], cacc[rt]);
    }
    STAMP(7);
    __builtin_amdgcn_s_setprio(SIDE_PRIO3);
    {
      LAS float* part = (LAS float*)(lds + K2_PART + sw * 8192);
#pragma unroll
      for (int rt = 0; rt < 8; ++rt)
#pragma unroll
        for (int r = 0; r < 4; ++r) part[256 * rt + 64 * g + 16 * r + fr] = cacc[rt][r];
    }
    group_sync(lds, w, 5, 0xf, lane);
    STAMP(8);
    {
      const int tt = sw * 64 + lane;
      unsigned yb[2][4];
#pragma unroll
      for (int c2 = 0; c2 < 2; ++c2) {
        LAS f32x4* q0 = (LAS f32x4*)(lds + K2_PART + (2 * c2) * 8192 + tt * 32);
        LAS f32x4* q1 = (LAS f32x4*)(lds + K2_PART + (2 * c2 + 1) * 8192 + tt * 32);
        const f32x4 s0 = q0[0] + q1[0], s1 = q0[1] + q1[1];
        const u32x4 uv = *(LAS u32x4*)(lds + K2_U + c2 * 4608 + 512 + tt * 16);
        const float dsk = c2 ? bfhi(dk2) : bflo(dk2);
#pragma unroll
        for (int e = 0; e < 4; ++e) {
          const f32x2 y = rb2(e < 2 ? (f32x2){s0[2 * e], s0[2 * e + 1]} : (f32x2){s1[2 * e - 4], s1[2 * e - 3]});
          const f32x2 ud = rb2((f32x2){bflo(uv[e]) * dsk, bfhi(uv[e]) * dsk});
          yb[c2][e] = pk2(y[0] + ud[0], y[1] + ud[1]);
        }
      }
      u32x4 o0, o1;
#pragma unroll
      for (int e = 0; e < 2; ++e) {
        o0[2 * e] = (yb[0][e] & 0xffffu) | (yb[1][e] << 16); o0[2 * e + 1] = (yb[0][e] >> 16) | (yb[1][e] & 0xffff0000u);
        o1[2 * e] = (yb[0][2 + e] & 0xffffu) | (yb[1][2 + e] << 16); o1[2 * e + 1] = (yb[0][2 + e] >> 16) | (yb[1][2 + e] & 0xffff0000u);
      }
      __builtin_amdgcn_raw_buffer_store_b128(o0, yrs, b * 8192 + tt * 32, 0, 16);
      __builtin_amdgcn_raw_buffer_store_b128(o1, yrs, b * 8192 + tt * 32 + 16, 0, 16);
      WAIT_V(0);
    }
    group_sync(lds, w, 6, 0xf, lane);
    if (sw == 0 && lane == 0) __hip_atomic_fetch_add(cnt + (256 + (b >> 5)) * 32, 1u, RLX_AGENT);
    STAMP(9);
#pragma unroll
    for (int n = 0; n < 12; ++n) { const int pc = 64 + sw * 12 + n; GLDS16(wsrc + (size_t)pc * 8192, wpiece(lds, pc)); }
    __builtin_amdgcn_s_setprio(0);
    STAMP(10);
  }
  const int wm = w & 1, wk = (w >> 1) & 3;
  const int er = wk >> 1, cg = wk & 1, ert = 2 * wm + er;
  const int t = 64 * ib + 16 * ert + fr, d0 = 64 * jb + 32 * cg + 8 * g;
  u32x4 hf[2][2][3]; u32x2 hh2[2][2];
  u32x4 xv = {0, 0, 0, 0};
  if (w < 8) {
#pragma unroll
    for (int s = 0; s < 2; ++s) {
      poll_ge(cnt + (ib * 8 + 2 * wk + s) * 32, 1u);
#pragma unroll
      for (int r = 0; r < 2; ++r) {
        const int hb = ((ib * 8 + 2 * wk + s) * 4 + 2 * wm + r) * 3584;
#pragma unroll
        for (int m = 0; m < 3; ++m) hf[r][s][m] = __builtin_amdgcn_raw_buffer_load_b128(hrs, hb + m * 1024 + lane * 16, 0, 16);
        hh2[r][s] = __builtin_amdgcn_raw_buffer_load_b64(hrs, hb + 3072 + lane * 8, 0, 16);
      }
    }
    xv = *(const u32x4*)(p.x + (size_t)t * 512 + d0);
  }
  WAIT_V(0);
  BAR();
  STAMP(11);
  if (w < 8) {
    f32x4 acc[2][4];
#pragma unroll
    for (int r = 0; r < 2; ++r)
#pragma unroll
      for (int c = 0; c < 4; ++c) acc[r][c] = (f32x4){0, 0, 0, 0};
    auto wadr = [&](int frow, int c) {
      const int row = frow + 4 * g + (fr >> 2);
      const int col = 32 * (c >> 1) + 8 * (fr & 3) + 4 * (c & 1);
      const int chunk = col >> 3;
      return wpiece(lds, frow >> 3) + (row - (frow & ~7)) * 128 + ((chunk ^ (2 * ((row >> 1) & 3))) * 16) + (col & 4) * 2;
    };
#pragma unroll
    for (int s = 0; s < 2; ++s) {
      const int f0 = 112 * (2 * wk + s);
#pragma unroll
      for (int m = 0; m < 3; ++m)
#pragma unroll
        for (int c = 0; c < 4; ++c) {
          const bf16x8 wf = cat8(trread(wadr(f0 + 32 * m, c)), trread(wadr(f0 + 32 * m + 16, c)));
#pragma unroll
          for (int r = 0; r < 2; ++r) acc[r][c] = mfma16(wf, __builtin_bit_cast(bf16x8, hf[r][s][m]), acc[r][c]);
        }
#pragma unroll
      for (int c = 0; c < 4; ++c) {
        const s16x4 wh = trread(wadr(f0 + 96, c));
#pragma unroll
        for (int r = 0; r < 2; ++r) acc[r][c] = __builtin_amdgcn_mfma_f32_16x16x16bf16_1k(wh, __builtin_bit_cast(s16x4, hh2[r][s]), acc[r][c], 0, 0, 0);
      }
    }
    STAMP(12);
    BAR();
    LAS f32x4* red = (LAS f32x4*)(lds + K2_RED3);
#pragma unroll
    for (int r = 0; r < 2; ++r)
#pragma unroll
      for (int c = 0; c < 4; ++c) red[(((wm * 2 + r) * 4 + c) * 4 + wk) * 64 + lane] = acc[r][c];
    poll_ge(cnt + (256 + jb) * 32, 32u);
    unsigned ya[4];
#pragma unroll
    for (int e = 0; e < 4; ++e) ya[e] = __builtin_amdgcn_raw_buffer_load_b32(yrs, ((d0 / 2 + e) * 2048 + t) * 4, 0, 16);
    BAR();
    f32x4 o[2];
#pragma unroll
    for (int n = 0; n < 2; ++n) {
      o[n] = red[(((wm * 2 + er) * 4 + 2 * cg + n) * 4 + 0) * 64 + lane];
#pragma unroll
      for (int k2 = 1; k2 < 4; ++k2) o[n] += red[(((wm * 2 + er) * 4 + 2 * cg + n) * 4 + k2) * 64 + lane];
    }
    u32x4 res;
#pragma unroll
    for (int n = 0; n < 2; ++n)
#pragma unroll
      for (int e = 0; e < 2; ++e) {
        const unsigned xw = xv[2 * n + e], yw = ya[2 * n + e];
        const f32x2 xa = rb2((f32x2){bflo(xw) + bflo(yw), bfhi(xw) + bfhi(yw)}), ob = rb2((f32x2){o[n][2 * e], o[n][2 * e + 1]});
        res[2 * n + e] = pk2(xa[0] + ob[0], xa[1] + ob[1]);
      }
    *(u32x4*)(p.out + (size_t)t * 512 + d0) = res;
  } else {
    BAR(); BAR();
  }
  STAMP(13);
}

extern "C" __global__ void zz_text_end() {}

extern "C" void kernel_launch(void* const* d_in, const int* in_sizes, int n_in, void* d_out, int out_size, void* d_ws, size_t ws_size,
                              hipStream_t stream) {
  (void)in_sizes; (void)n_in; (void)out_size; (void)ws_size;
  Params p;
  p.x = (const u16*)d_in[0]; p.a_re = (const u16*)d_in[1]; p.a_im = (const u16*)d_in[2]; p.b_re = (const u16*)d_in[3];
  p.b_im = (const u16*)d_in[4]; p.c_re = (const u16*)d_in[5]; p.c_im = (const u16*)d_in[6]; p.log_dt = (const u16*)d_in[7];
  p.dskip = (const u16*)d_in[8]; p.w_in = (const u16*)d_in[10]; p.w_sp = (const u16*)d_in[11]; p.b_sp = (const u16*)d_in[12];
  p.w_out = (const u16*)d_in[13]; p.out = (u16*)d_out; p.ws = (char*)d_ws;
  (void)hipFuncSetAttribute((const void*)k1_kernel, hipFuncAttributeMaxDynamicSharedMemorySize, K1_LDS);
  (void)hipFuncSetAttribute((const void*)k2_kernel, hipFuncAttributeMaxDynamicSharedMemorySize, K2_LDS);
  k1_kernel<<<256, 768, K1_LDS, stream>>>(p);
  k2_kernel<<<256, 1024, K2_LDS, stream>>>(p);
}
```
